```python
import torch

def _softmax(x, axis=-1):
    return torch.softmax(x, dim=axis)
def _gelu(x):
    return torch.nn.functional.gelu(x, approximate="tanh")
def _silu(x):
    return torch.nn.functional.silu(x)
def _sigmoid(x):
    return torch.sigmoid(x)
def _layernorm(x, axis=-1, eps=1e-5):
    m = x.mean(dim=axis, keepdim=True)
    v = x.var(dim=axis, keepdim=True, unbiased=False)
    return (x - m) / torch.sqrt(v + eps)
def _scatter_add(tgt, idx, src, axis=0):
    return tgt.clone().index_add_(axis, idx, src)
def _segment_sum(src, seg, n):
    out = torch.zeros((n,) + src.shape[1:], dtype=src.dtype)
    return out.index_add_(0, seg, src)
def _pad(x, pw):
    flat = tuple(v for pair in reversed(pw) for v in pair)
    return torch.nn.functional.pad(x, flat)

kernel_name = 'recurrent_scan__gqa_block_cached__glu_gated_swiglu__decode'

def setup_inputs():

    _w = (lambda a: (lambda t: t.to(torch.bfloat16) if t.is_floating_point() else t)(torch.as_tensor(a)))
    def _A_setup():
        B, Sq, D = 2, 1, 512
        Hq, Hkv, Sk, Dh = 32, 8, 32768, 128
        assert Hq % Hkv == 0, (Hq, Hkv)
        return {
            "x": _w((lambda *s: torch.randn(*s, dtype=torch.bfloat16) * 0.4)(B, Sq, D)),
            "k_cache": _w((lambda *s: torch.randn(*s, dtype=torch.bfloat16) * 1.6)(B, Hkv, Sk, Dh)),
            "v_cache": _w((lambda *s: torch.randn(*s, dtype=torch.bfloat16) * 1.6)(B, Hkv, Sk, Dh)),
            "wq": _w((lambda *s: torch.randn(*s, dtype=torch.bfloat16) * 1.6)(D, Hq * Dh) * (D ** -0.5)),
            "wo": _w((lambda *s: torch.randn(*s, dtype=torch.bfloat16) * 1.6)(Hq * Dh, D) * ((Hq * Dh) ** -0.5)),
        }
    _A_in = _A_setup()

    def _B_setup():
        B, S, D, F = 2, 1, 512, 1792
        return {
            "x": _w((lambda *s: torch.randn(*s, dtype=torch.bfloat16) * 0.4)(B, S, D)),
            "w1": _w((lambda *s: torch.randn(*s, dtype=torch.bfloat16) * 1.6)(D, F) * (2.0 / D) ** 0.5),
            "w3": _w((lambda *s: torch.randn(*s, dtype=torch.bfloat16) * 1.6)(D, F) * (2.0 / D) ** 0.5),
            "w2": _w((lambda *s: torch.randn(*s, dtype=torch.bfloat16) * 1.6)(F, D) * (2.0 / F) ** 0.5),
        }
    _B_in = _B_setup()

    return {
        "A_x": _A_in["x"],
        "A_k_cache": _A_in["k_cache"],
        "A_v_cache": _A_in["v_cache"],
        "A_wq": _A_in["wq"],
        "A_wo": _A_in["wo"],
        "B_x": _B_in["x"],
        "B_w1": _B_in["w1"],
        "B_w3": _B_in["w3"],
        "B_w2": _B_in["w2"],
    }

def reference(A_x, A_k_cache, A_v_cache, A_wq, A_wo, B_x, B_w1, B_w3, B_w2):
    def _A_ref(x, k_cache, v_cache, wq, wo):
        B, Sq, D = x.shape
        Hq, Dh = 32, 128
        Hkv = k_cache.shape[1]
        rep = Hq // Hkv
        q = (lambda x, *d: x.permute(*d))((x @ wq).reshape(B, Sq, Hq, Dh), 0, 2, 1, 3)
        k = (lambda x, n, axis: torch.repeat_interleave(x, n, dim=axis))(k_cache, rep, axis=1)
        v = (lambda x, n, axis: torch.repeat_interleave(x, n, dim=axis))(v_cache, rep, axis=1)
        scores = torch.einsum("bhqd,bhkd->bhqk", q, k) * (Dh ** -0.5)
        p = _softmax(scores, axis=-1)
        o = torch.einsum("bhqk,bhkd->bhqd", p, v)
        return (lambda x, *d: x.permute(*d))(o, 0, 2, 1, 3).reshape(B, Sq, Hq * Dh) @ wo

    def _B_ref(x, w1, w3, w2):
        gate = torch.einsum("bsd,df->bsf", x, w1)
        up = torch.einsum("bsd,df->bsf", x, w3)
        h = _silu(gate) * up
        out = torch.einsum("bsf,fd->bsd", h, w2)
        return out

    x = A_x
    xn = _layernorm(x, axis=-1)
    s = _A_ref(xn, A_k_cache, A_v_cache, A_wq, A_wo)
    g = _silu(_B_ref(xn, B_w1, B_w3, B_w2))
    return x + s * g
```

```cpp
#include <hip/hip_runtime.h>
#include <hip/hip_bf16.h>
#include <stdint.h>

#define GAS __attribute__((address_space(1)))
#define LAS __attribute__((address_space(3)))
typedef unsigned v4u __attribute__((ext_vector_type(4)));
typedef unsigned v2u __attribute__((ext_vector_type(2)));
typedef float v4f __attribute__((ext_vector_type(4)));
typedef __bf16 v8bf __attribute__((ext_vector_type(8)));
typedef short v4s __attribute__((ext_vector_type(4)));
typedef unsigned short u16;

constexpr int DM = 512, HQ = 32, HKV = 8, SK = 32768, DH = 128, FF = 1792;
constexpr int NWG = 256, NSPLIT = 16;
constexpr float C2 = 0.088388347648318440f * 1.4426950408889634f;

__device__ __forceinline__ float bf2f(u16 v) { return __uint_as_float((unsigned)v << 16); }
__device__ __forceinline__ u16 f2bf(float f) { __bf16 b = (__bf16)f; return __builtin_bit_cast(u16, b); }
__device__ __forceinline__ float rbf(float f) { return bf2f(f2bf(f)); }
__device__ __forceinline__ float xrow16_max(float x) {
  auto s = __builtin_amdgcn_permlane16_swap(__float_as_uint(x), __float_as_uint(x), false, false);
  x = fmaxf(__uint_as_float(s[0]), __uint_as_float(s[1]));
  auto t = __builtin_amdgcn_permlane32_swap(__float_as_uint(x), __float_as_uint(x), false, false);
  return fmaxf(__uint_as_float(t[0]), __uint_as_float(t[1]));
}
__device__ __forceinline__ float xrow16_sum(float x) {
  auto s = __builtin_amdgcn_permlane16_swap(__float_as_uint(x), __float_as_uint(x), false, false);
  x = __uint_as_float(s[0]) + __uint_as_float(s[1]);
  auto t = __builtin_amdgcn_permlane32_swap(__float_as_uint(x), __float_as_uint(x), false, false);
  return __uint_as_float(t[0]) + __uint_as_float(t[1]);
}
__device__ __forceinline__ float wave_sum(float v) {
#pragma unroll
  for (int m = 32; m >= 1; m >>= 1) v += __shfl_xor(v, m);
  return v;
}
__device__ __forceinline__ float silu_f(float x) { return x / (1.f + __expf(-x)); }

constexpr size_t WS_Q = 0;
constexpr size_t WS_H = 16384;
constexpr size_t WS_PML = 65536;
constexpr size_t WS_PO = 131072;
constexpr size_t WS_Y = WS_PO + 256 * 512 * 4;
constexpr size_t WS_Z = WS_Y + 256 * 512 * 4;

__global__ void __launch_bounds__(256) k_prep(const u16* __restrict__ x, const u16* __restrict__ wq, const u16* __restrict__ w1, const u16* __restrict__ w3, unsigned char* ws) {
  __shared__ float xn[2][DM];
  __shared__ float red[256][17];
  const int tid = threadIdx.x, lane = tid & 63, wave = tid >> 6;
  if (wave < 2) {
    const int b = wave;
    float v[8];
    for (int j = 0; j < 8; ++j) v[j] = bf2f(x[b * DM + lane * 8 + j]);
    float s = 0; for (int j = 0; j < 8; ++j) s += v[j];
    const float mean = wave_sum(s) * (1.f / DM);
    float q = 0; for (int j = 0; j < 8; ++j) { const float d = v[j] - mean; q += d * d; }
    const float var = wave_sum(q) * (1.f / DM);
    const float mb = rbf(mean), vb = rbf(var);
    const float den = rbf(sqrtf(rbf(vb + 1e-5f)));
    for (int j = 0; j < 8; ++j) xn[b][lane * 8 + j] = rbf(rbf(v[j] - mb) / den);
  }
  __syncthreads();
  const int j = blockIdx.x;
  {
    float acc[2][16];
    for (int c = 0; c < 16; ++c) acc[0][c] = acc[1][c] = 0.f;
    for (int rr = 0; rr < 2; ++rr) { const int r = tid * 2 + rr;
      for (int c = 0; c < 16; ++c) { const float w = bf2f(wq[(size_t)r * 4096 + 16 * j + c]); acc[0][c] += xn[0][r] * w; acc[1][c] += xn[1][r] * w; } }
    for (int b = 0; b < 2; ++b) {
      for (int c = 0; c < 16; ++c) red[tid][c] = acc[b][c];
      __syncthreads();
      if (tid < 16) { float s = 0; for (int t = 0; t < 256; ++t) s += red[t][tid]; ((u16*)(ws + WS_Q))[b * 4096 + 16 * j + tid] = f2bf(s); }
      __syncthreads();
    }
  }
  if (j < 224) {
    float acc[2][16];
    for (int c = 0; c < 16; ++c) acc[0][c] = acc[1][c] = 0.f;
    for (int rr = 0; rr < 2; ++rr) { const int r = tid * 2 + rr;
      for (int c = 0; c < 8; ++c) { const float a = bf2f(w1[(size_t)r * FF + 8 * j + c]), u = bf2f(w3[(size_t)r * FF + 8 * j + c]);
        acc[0][c] += xn[0][r] * a; acc[1][c] += xn[1][r] * a; acc[0][8 + c] += xn[0][r] * u; acc[1][8 + c] += xn[1][r] * u; } }
    for (int b = 0; b < 2; ++b) {
      for (int c = 0; c < 16; ++c) red[tid][c] = acc[b][c];
      __syncthreads();
      if (tid < 8) { float g = 0, u = 0; for (int t = 0; t < 256; ++t) { g += red[t][tid]; u += red[t][8 + tid]; }
        g = rbf(g); u = rbf(u); ((float*)(ws + WS_H))[b * FF + 8 * j + tid] = rbf(rbf(silu_f(g)) * u); }
      __syncthreads();
    }
  }
}

constexpr int SW = 4;
constexpr int RSLOT = 4;
constexpr int TPW = SK / NSPLIT / 16 / SW;
constexpr int RING_BYTES = SW * RSLOT * 8192;

#define DMA_TILE(kb, vb, ldsaddr)                                                              \
  asm volatile("s_mov_b32 %0, m0\n\t"                                                           \
               "s_mov_b32 m0, %7\n\t"                                                           \
               "s_nop 0\n\t"                                                                    \
               "global_load_lds_dwordx4 %1, %5 nt\n\t"                                          \
               "global_load_lds_dwordx4 %2, %5 offset:1024 nt\n\t"                              \
               "global_load_lds_dwordx4 %1, %5 offset:2048 nt\n\t"                              \
               "global_load_lds_dwordx4 %2, %5 offset:3072 nt\n\t"                              \
               "s_add_u32 m0, m0, 0x1000\n\t"                                                   \
               "s_nop 0\n\t"                                                                    \
               "global_load_lds_dwordx4 %3, %6 nt\n\t"                                          \
               "global_load_lds_dwordx4 %4, %6 offset:1024 nt\n\t"                              \
               "global_load_lds_dwordx4 %3, %6 offset:2048 nt\n\t"                              \
               "global_load_lds_dwordx4 %4, %6 offset:3072 nt\n\t"                              \
               "s_mov_b32 m0, %0"                                                               \
               : "=&s"(m0keep) : "v"(koe), "v"(koo), "v"(voe), "v"(voo), "s"(kb), "s"(vb), "s"(ldsaddr) : "memory", "scc")

__global__ void __launch_bounds__(256, 1) k_attn(const u16* __restrict__ Kc, const u16* __restrict__ Vc, unsigned char* ws) {
  extern __shared__ __attribute__((aligned(16))) unsigned char lds[];
  const int tid = threadIdx.x, lane = tid & 63, wave = __builtin_amdgcn_readfirstlane(tid >> 6);
  const int wg = blockIdx.x, grp = wg / NSPLIT, sp = wg % NSPLIT;
  const int b = grp / HKV, kvh = grp % HKV;
  const int n = lane & 15, g = lane >> 4;
  const unsigned ldsbase = (unsigned)(uintptr_t)(LAS unsigned char*)lds;
  const unsigned ring = ldsbase + wave * (RSLOT * 8192);
  const unsigned r4 = lane >> 4, pos = lane & 15;
  const unsigned koe = r4 * 256 + ((pos & 8) | ((pos & 7) ^ r4)) * 16;
  const unsigned koo = r4 * 256 + ((pos & 8) | ((pos & 7) ^ (4 + r4))) * 16;
  const unsigned voe = r4 * 256 + (pos ^ (r4 << 1)) * 16;
  const unsigned voo = r4 * 256 + (pos ^ ((4 + r4) << 1)) * 16;
  const size_t gbase = ((size_t)grp * SK + (size_t)sp * (SK / NSPLIT)) * 256 + (size_t)wave * 4096;
  const char* kptr = (const char*)Kc + gbase;
  const char* vptr = (const char*)Vc + gbase;
  unsigned m0keep;
  { const char* k0 = kptr; const char* v0 = vptr; DMA_TILE(k0, v0, ring); }
  { const char* k0 = kptr + SW * 4096; const char* v0 = vptr + SW * 4096; const unsigned l1 = ring + 8192; DMA_TILE(k0, v0, l1); }
  v8bf qf[4];
  {
    const u16* q = (const u16*)(ws + WS_Q) + b * 4096 + (kvh * 4 + (n & 3)) * DH;
#pragma unroll
    for (int t = 0; t < 4; ++t) {
      const int c = 8 * (g & 1) + 4 * (g >> 1) + t;
      v4u v; asm volatile("global_load_dwordx4 %0, %1, off\n\ts_waitcnt vmcnt(0)" : "=&v"(v) : "v"(q + 8 * c) : "memory");
      if (n >= 4) v = v4u{0, 0, 0, 0};
      qf[t] = __builtin_bit_cast(v8bf, v);
    }
  }
  unsigned ka[4], va[8];
#pragma unroll
  for (int t = 0; t < 4; ++t) ka[t] = ring + n * 256 + ((8 * (g & 1)) | ((4 * (g >> 1) + t) ^ (n & 7))) * 16;
  { const unsigned q4 = (lane >> 2) & 3, p = lane & 3, row = 4 * g + q4;
#pragma unroll
    for (int dt = 0; dt < 8; ++dt) va[dt] = ring + 4096 + row * 256 + ((dt ^ (row & 7)) << 5) + p * 8; }
  float m = -INFINITY, l = 0.f;
  v4f O[8];
#pragma unroll
  for (int dt = 0; dt < 8; ++dt) O[dt] = v4f{0, 0, 0, 0};

  for (int i0 = 0; i0 < TPW; i0 += 2) {
#pragma unroll
    for (int u = 0; u < 2; ++u) {
      const int i = i0 + u;
      if (i + 1 < TPW) asm volatile("s_waitcnt vmcnt(8)" ::: "memory"); else asm volatile("s_waitcnt vmcnt(0)" ::: "memory");
      const unsigned so = u * 8192;
      v4u kf[4]; v4s vf[8];
#pragma unroll
      for (int t = 0; t < 4; ++t) kf[t] = *(const LAS v4u*)(uintptr_t)(ka[t] + so);
#pragma unroll
      for (int dt = 0; dt < 8; ++dt) vf[dt] = __builtin_amdgcn_ds_read_tr16_b64_v4i16((LAS v4s*)(uintptr_t)(va[dt] + so));
      asm volatile("s_waitcnt lgkmcnt(0)" ::: "memory");
#pragma unroll
      for (int t = 0; t < 4; ++t) asm volatile("" : "+v"(kf[t]));
#pragma unroll
      for (int dt = 0; dt < 8; ++dt) asm volatile("" : "+v"(vf[dt]));
      if (i + 2 < TPW) { const char* k0 = kptr + (size_t)(i + 2) * (SW * 4096); const char* v0 = vptr + (size_t)(i + 2) * (SW * 4096); const unsigned l1 = ring + so; DMA_TILE(k0, v0, l1); }
      v4f S = {0, 0, 0, 0};
#pragma unroll
      for (int t = 0; t < 4; ++t) S = __builtin_amdgcn_mfma_f32_16x16x32_bf16(__builtin_bit_cast(v8bf, kf[t]), qf[t], S, 0, 0, 0);
      float tm = fmaxf(fmaxf(S[0], S[1]), fmaxf(S[2], S[3]));
      tm = xrow16_max(tm) * C2;
      if (__any(tm > m)) {
        const float mn = fmaxf(m, tm);
        const float alpha = __builtin_amdgcn_exp2f(m - mn);
        l *= alpha;
#pragma unroll
        for (int dt = 0; dt < 8; ++dt) O[dt] *= alpha;
        m = mn;
      }
      const float p0 = __builtin_amdgcn_exp2f(fmaf(S[0], C2, -m)), p1 = __builtin_amdgcn_exp2f(fmaf(S[1], C2, -m));
      const float p2 = __builtin_amdgcn_exp2f(fmaf(S[2], C2, -m)), p3 = __builtin_amdgcn_exp2f(fmaf(S[3], C2, -m));
      l += (p0 + p1) + (p2 + p3);
      const v4s pf = {(short)f2bf(p0), (short)f2bf(p1), (short)f2bf(p2), (short)f2bf(p3)};
#pragma unroll
      for (int dt = 0; dt < 8; ++dt) O[dt] = __builtin_amdgcn_mfma_f32_16x16x16bf16_1k(vf[dt], pf, O[dt], 0, 0, 0);
    }
  }
  l = xrow16_sum(l);
  __syncthreads();
  float* rec = (float*)lds;
  if (n < 4) {
    float* r = rec + (wave * 4 + n) * 132;
#pragma unroll
    for (int dt = 0; dt < 8; ++dt) *(v4f*)(r + 4 + 16 * dt + 4 * g) = O[dt];
    if (g == 0) { r[0] = m; r[1] = l; }
  }
  __syncthreads();
  for (int e = tid; e < 512; e += 256) {
    const int h = e >> 7, d = e & 127;
    float M = -INFINITY;
#pragma unroll
    for (int w = 0; w < 4; ++w) M = fmaxf(M, rec[(w * 4 + h) * 132]);
    float L = 0, o = 0;
#pragma unroll
    for (int w = 0; w < 4; ++w) { const float* r = rec + (w * 4 + h) * 132; const float wt = __builtin_amdgcn_exp2f(r[0] - M); L = fmaf(wt, r[1], L); o = fmaf(wt, r[4 + d], o); }
    ((float*)(ws + WS_PO))[(size_t)wg * 512 + e] = o;
    if (d == 0) { ((float*)(ws + WS_PML))[(wg * 4 + h) * 2] = M; ((float*)(ws + WS_PML))[(wg * 4 + h) * 2 + 1] = L; }
  }
}

__global__ void __launch_bounds__(256) k_comb(const u16* __restrict__ wo, const u16* __restrict__ w2, unsigned char* ws) {
  __shared__ float o[32];
  __shared__ float hh[2][8];
  const int tid = threadIdx.x, wg = blockIdx.x, grp = wg / NSPLIT, sp = wg % NSPLIT, kvh = grp % HKV;
  const int h = sp >> 2, d0 = (sp & 3) * 32;
  const float* pml = (const float*)(ws + WS_PML); const float* po = (const float*)(ws + WS_PO);
  if (tid < 32) {
    float M = -INFINITY;
    for (int s = 0; s < 16; ++s) M = fmaxf(M, pml[((grp * 16 + s) * 4 + h) * 2]);
    float L = 0, acc = 0;
    for (int s = 0; s < 16; ++s) { const float wt = __builtin_amdgcn_exp2f(pml[((grp * 16 + s) * 4 + h) * 2] - M); L = fmaf(wt, pml[((grp * 16 + s) * 4 + h) * 2 + 1], L); acc = fmaf(wt, po[(size_t)(grp * 16 + s) * 512 + h * 128 + d0 + tid], acc); }
    o[tid] = rbf(acc / L);
  }
  if (tid >= 64 && tid < 78) { const int t = tid - 64; hh[t / 7][t % 7] = ((const float*)(ws + WS_H))[(t / 7) * FF + wg * 7 + t % 7]; }
  __syncthreads();
  const int row0 = (kvh * 4 + h) * 128 + d0;
  for (int c = tid; c < 512; c += 256) {
    float y = 0; for (int r = 0; r < 32; ++r) y = fmaf(o[r], bf2f(wo[(size_t)(row0 + r) * 512 + c]), y);
    ((float*)(ws + WS_Y))[(size_t)wg * 512 + c] = y;
    float z0 = 0, z1 = 0; for (int r = 0; r < 7; ++r) { const float w = bf2f(w2[(size_t)(wg * 7 + r) * 512 + c]); z0 = fmaf(hh[0][r], w, z0); z1 = fmaf(hh[1][r], w, z1); }
    ((float*)(ws + WS_Z))[(size_t)wg * 1024 + c] = z0; ((float*)(ws + WS_Z))[(size_t)wg * 1024 + 512 + c] = z1;
  }
}
__global__ void __launch_bounds__(64) k_final(const u16* __restrict__ x, unsigned char* ws, u16* out) {
  const int e = blockIdx.x * 4 + (threadIdx.x & 3), part = threadIdx.x >> 2;
  const int b = e >> 9, c = e & 511;
  const float* Y = (const float*)(ws + WS_Y); const float* Z = (const float*)(ws + WS_Z);
  float s = 0, z = 0;
  for (int i = part; i < 128; i += 16) s += Y[(size_t)(b * 128 + i) * 512 + c];
  for (int i = part; i < 256; i += 16) z += Z[(size_t)i * 1024 + b * 512 + c];
  for (int mk = 4; mk < 64; mk <<= 1) { s += __shfl_xor(s, mk); z += __shfl_xor(z, mk); }
  if (part == 0) { const float gg = rbf(silu_f(rbf(z))); out[e] = f2bf(bf2f(x[e]) + rbf(rbf(s) * gg)); }
}

extern "C" void kernel_launch(void* const* d_in, const int* in_sizes, int n_in, void* d_out, int out_size, void* d_ws, size_t ws_size, hipStream_t stream) {
  const u16* x = (const u16*)d_in[0]; const u16* K = (const u16*)d_in[1]; const u16* V = (const u16*)d_in[2];
  const u16* wq = (const u16*)d_in[3]; const u16* wo = (const u16*)d_in[4];
  const u16* w1 = (const u16*)d_in[6]; const u16* w3 = (const u16*)d_in[7]; const u16* w2 = (const u16*)d_in[8];
  unsigned char* ws = (unsigned char*)d_ws;
  (void)hipFuncSetAttribute((const void*)k_attn, hipFuncAttributeMaxDynamicSharedMemorySize, RING_BYTES);
  k_prep<<<256, 256, 0, stream>>>(x, wq, w1, w3, ws);
  k_attn<<<256, 256, RING_BYTES, stream>>>(K, V, ws);
  k_comb<<<256, 256, 0, stream>>>(wo, w2, ws);
  k_final<<<256, 64, 0, stream>>>(x, ws, (u16*)d_out);
}
```

```cpp
#include <hip/hip_runtime.h>
#include <hip/hip_bf16.h>
#include <stdint.h>

#define GAS __attribute__((address_space(1)))
#define LAS __attribute__((address_space(3)))
typedef unsigned v4u __attribute__((ext_vector_type(4)));
typedef unsigned v2u __attribute__((ext_vector_type(2)));
typedef float v4f __attribute__((ext_vector_type(4)));
typedef float v2f __attribute__((ext_vector_type(2)));
typedef __bf16 v8bf __attribute__((ext_vector_type(8)));
typedef __bf16 v2bf __attribute__((ext_vector_type(2)));
typedef short v4s __attribute__((ext_vector_type(4)));
typedef short v8s __attribute__((ext_vector_type(8)));
typedef unsigned short u16;
typedef unsigned long long u64;

constexpr int DM = 512, HKV = 8, SK = 32768, FF = 1792;
constexpr int NWG = 256, NSPLIT = 16;
constexpr float SCALE = 0.088388347648318440f, LOG2E = 1.4426950408889634f;
constexpr int SW = 4, RSLOT = 4;
constexpr int TPW = SK / NSPLIT / 16 / SW;

constexpr int L_RING = 0, L_STAGE = SW * RSLOT * 8192, L_XN = L_STAGE + 16384, L_QL = L_XN + 2048, L_MISC = L_QL + 1024, LDS_BYTES = L_MISC + 256;
constexpr unsigned W_EPOCH = 0, W_QG = 4096, W_HG = W_QG + 2 * 4096 * 8, W_IN1 = W_HG + 32768, W_INY = W_IN1 + NWG * 16 * 320, W_INZ = W_INY + NWG * 4096, W_END = W_INZ + NWG * 8192;
constexpr unsigned SPIN_CAP = 1u << 16;

__device__ __forceinline__ float bf2f(unsigned v) { return __uint_as_float(v << 16); }
__device__ __forceinline__ u16 f2bf(float f) { __bf16 b = (__bf16)f; return __builtin_bit_cast(u16, b); }
__device__ __forceinline__ float rbf(float f) { return bf2f(f2bf(f)); }
__device__ __forceinline__ unsigned pk2(float lo, float hi) { v2bf p = {(__bf16)lo, (__bf16)hi}; return __builtin_bit_cast(unsigned, p); }
__device__ __forceinline__ float lo16(unsigned v) { return __uint_as_float(v << 16); }
__device__ __forceinline__ float hi16(unsigned v) { return __uint_as_float(v & 0xffff0000u); }
template <int CTRL> __device__ __forceinline__ float dpp(float x) {
  return __builtin_bit_cast(float, __builtin_amdgcn_mov_dpp(__builtin_bit_cast(int, x), CTRL, 0xf, 0xf, true));
}
constexpr int XOR1 = 0xB1, XOR2 = 0x4E, HMIR = 0x141, MIR = 0x140, ROR2 = 0x122, ROR4 = 0x124, ROR8 = 0x128;
__device__ __forceinline__ float xrow16_max(float x) {
  auto s = __builtin_amdgcn_permlane16_swap(__float_as_uint(x), __float_as_uint(x), false, false);
  x = fmaxf(__uint_as_float(s[0]), __uint_as_float(s[1]));
  auto t = __builtin_amdgcn_permlane32_swap(__float_as_uint(x), __float_as_uint(x), false, false);
  return fmaxf(__uint_as_float(t[0]), __uint_as_float(t[1]));
}
__device__ __forceinline__ float xrow16_sum(float x) {
  auto s = __builtin_amdgcn_permlane16_swap(__float_as_uint(x), __float_as_uint(x), false, false);
  x = __uint_as_float(s[0]) + __uint_as_float(s[1]);
  auto t = __builtin_amdgcn_permlane32_swap(__float_as_uint(x), __float_as_uint(x), false, false);
  return __uint_as_float(t[0]) + __uint_as_float(t[1]);
}
__device__ __forceinline__ float row_sum(float x) { x += dpp<XOR1>(x); x += dpp<XOR2>(x); x += dpp<HMIR>(x); x += dpp<MIR>(x); return x; }
__device__ __forceinline__ float row_max(float x) { x = fmaxf(x, dpp<XOR1>(x)); x = fmaxf(x, dpp<XOR2>(x)); x = fmaxf(x, dpp<HMIR>(x)); x = fmaxf(x, dpp<MIR>(x)); return x; }
__device__ __forceinline__ float wave_sum(float x) { return xrow16_sum(row_sum(x)); }
__device__ __forceinline__ float parity_sum(float x) { x += dpp<ROR2>(x); x += dpp<ROR4>(x); x += dpp<ROR8>(x); return xrow16_sum(x); }
__device__ __forceinline__ float silu_f(float x) { return x * __builtin_amdgcn_rcpf(1.f + __builtin_amdgcn_exp2f(-x * LOG2E)); }
__device__ __forceinline__ float dot2(unsigned a, unsigned b, float c) { return __builtin_amdgcn_fdot2_f32_bf16(__builtin_bit_cast(v2bf, a), __builtin_bit_cast(v2bf, b), c, false); }

struct Tag { unsigned lo, hi; };
__device__ __forceinline__ v4u gran2(float a, float b, Tag t) { return v4u{(__float_as_uint(a) >> 8) | t.lo, t.hi, (__float_as_uint(b) >> 8) | t.lo, t.hi}; }
__device__ __forceinline__ bool gok2(v4u v, Tag t) { return (v.y == t.hi) & (v.w == t.hi) & (((v.x ^ t.lo) | (v.z ^ t.lo)) < 0x1000000u); }
__device__ __forceinline__ float gval(unsigned lo) { return __uint_as_float(lo << 8); }
#define BLOAD16(off) __builtin_amdgcn_raw_buffer_load_b128(rs, (off), 0, 16)
#define BLOAD8(off) __builtin_amdgcn_raw_buffer_load_b64(rs, (off), 0, 16)
#define BSTORE16(v, off) __builtin_amdgcn_raw_buffer_store_b128((v), rs, (off), 0, 16)
#define MEMFENCE() asm volatile("" ::: "memory")
#define BARRIER() do { asm volatile("s_waitcnt lgkmcnt(0)" ::: "memory"); __builtin_amdgcn_s_barrier(); MEMFENCE(); } while (0)

#define DMA_TILE(kb, vb, ldsaddr)                                                              \
  asm volatile("s_mov_b32 %0, m0\n\t"                                                           \
               "s_mov_b32 m0, %7\n\t"                                                           \
               "s_nop 0\n\t"                                                                    \
               "global_load_lds_dwordx4 %1, %5 nt\n\t"                                          \
               "global_load_lds_dwordx4 %2, %5 offset:1024 nt\n\t"                              \
               "global_load_lds_dwordx4 %1, %5 offset:2048 nt\n\t"                              \
               "global_load_lds_dwordx4 %2, %5 offset:3072 nt\n\t"                              \
               "s_add_u32 m0, m0, 0x1000\n\t"                                                   \
               "s_nop 0\n\t"                                                                    \
               "global_load_lds_dwordx4 %3, %6 nt\n\t"                                          \
               "global_load_lds_dwordx4 %4, %6 offset:1024 nt\n\t"                              \
               "global_load_lds_dwordx4 %3, %6 offset:2048 nt\n\t"                              \
               "global_load_lds_dwordx4 %4, %6 offset:3072 nt\n\t"                              \
               "s_mov_b32 m0, %0"                                                               \
               : "=&s"(m0keep) : "v"(koe), "v"(koo), "v"(voe), "v"(voo), "s"(kb), "s"(vb), "s"(ldsaddr) : "memory", "scc")

__device__ __forceinline__ void dma1(const void* gsrc, unsigned lds_dst) {
  unsigned keep;
  asm volatile("s_mov_b32 %0, m0\n\ts_mov_b32 m0, %2\n\ts_nop 0\n\tglobal_load_lds_dwordx4 %1, off nt\n\ts_mov_b32 m0, %0" : "=&s"(keep) : "v"(gsrc), "s"(lds_dst) : "memory");
}

__device__ __forceinline__ v4f proj16(unsigned ldsbase, int lane) {
  const unsigned aaddr = ldsbase + L_STAGE + lane * 8;
  const unsigned baddr = ldsbase + L_XN + (lane & 1) * 1024 + (lane >> 4) * 8;
  v4f acc[4];
#pragma unroll
  for (int i = 0; i < 4; ++i) acc[i] = v4f{0, 0, 0, 0};
#pragma unroll
  for (int s = 0; s < 16; ++s) {
    const v4s a0 = __builtin_amdgcn_ds_read_tr16_b64_v4i16((LAS v4s*)(uintptr_t)(aaddr + s * 1024));
    const v4s a1 = __builtin_amdgcn_ds_read_tr16_b64_v4i16((LAS v4s*)(uintptr_t)(aaddr + s * 1024 + 512));
    const v4s b0 = *(const LAS v4s*)(uintptr_t)(baddr + s * 64);
    const v4s b1 = *(const LAS v4s*)(uintptr_t)(baddr + s * 64 + 32);
    const v8s a = __builtin_shufflevector(a0, a1, 0, 1, 2, 3, 4, 5, 6, 7);
    const v8s bb = __builtin_shufflevector(b0, b1, 0, 1, 2, 3, 4, 5, 6, 7);
    acc[s & 3] = __builtin_amdgcn_mfma_f32_16x16x32_bf16(__builtin_bit_cast(v8bf, a), __builtin_bit_cast(v8bf, bb), acc[s & 3], 0, 0, 0);
  }
  return (acc[0] + acc[1]) + (acc[2] + acc[3]);
}

__global__ void __launch_bounds__(512, 2) k_fused(const u16* __restrict__ x, const u16* __restrict__ Kc, const u16* __restrict__ Vc, const u16* __restrict__ wq,
                                                  const u16* __restrict__ wo, const u16* __restrict__ w1, const u16* __restrict__ w3, const u16* __restrict__ w2,
                                                  u16* __restrict__ out, unsigned char* __restrict__ ws) {
  extern __shared__ __attribute__((aligned(16))) unsigned char lds[];
  const int tid = threadIdx.x, lane = tid & 63, wave = __builtin_amdgcn_readfirstlane(tid >> 6);
  const int wg = blockIdx.x, grp = wg >> 4, sp = wg & 15, b = grp >> 3, kvh = grp & 7;
  const unsigned ldsbase = (unsigned)(uintptr_t)(LAS unsigned char*)lds;
  const __amdgpu_buffer_rsrc_t rs = __builtin_amdgcn_make_buffer_rsrc(ws, 0, W_END, 0x00020000);
  const int n = lane & 15, g = lane >> 4;

  if (wave < SW) {
    const unsigned ring = ldsbase + L_RING + wave * (RSLOT * 8192);
    const unsigned r4 = lane >> 4, pos = lane & 15;
    const unsigned koe = r4 * 256 + ((pos & 8) | ((pos & 7) ^ r4)) * 16;
    const unsigned koo = r4 * 256 + ((pos & 8) | ((pos & 7) ^ (4 + r4))) * 16;
    const unsigned voe = r4 * 256 + (pos ^ (r4 << 1)) * 16;
    const unsigned voo = r4 * 256 + (pos ^ ((4 + r4) << 1)) * 16;
    const size_t gbase = ((size_t)grp * SK + (size_t)sp * (SK / NSPLIT)) * 256 + (size_t)wave * 4096;
    const char* kptr = (const char*)Kc + gbase;
    const char* vptr = (const char*)Vc + gbase;
    unsigned m0keep;
#define ISSUE(t, slot) do { const char* k0_ = kptr + (size_t)(t) * (SW * 4096); const char* v0_ = vptr + (size_t)(t) * (SW * 4096); const unsigned l_ = ring + (slot) * 8192; DMA_TILE(k0_, v0_, l_); } while (0)
    ISSUE(0, 0); ISSUE(1, 1);
    asm volatile("s_waitcnt vmcnt(8)" ::: "memory");
    ISSUE(2, 2);
    asm volatile("s_waitcnt vmcnt(8)" ::: "memory");
    ISSUE(3, 3);
    unsigned ka[4], va[8];
#pragma unroll
    for (int t = 0; t < 4; ++t) ka[t] = ring + n * 256 + ((8 * (g & 1)) | ((4 * (g >> 1) + t) ^ (n & 7))) * 16;
    { const unsigned q4 = (lane >> 2) & 3, p = lane & 3, row = 4 * g + q4;
#pragma unroll
      for (int dt = 0; dt < 8; ++dt) va[dt] = ring + 4096 + row * 256 + ((dt ^ (row & 7)) << 5) + p * 8; }
    BARRIER();
    v8bf qf[4];
#pragma unroll
    for (int t = 0; t < 4; ++t) qf[t] = __builtin_bit_cast(v8bf, *(const LAS v4u*)(uintptr_t)(ldsbase + L_QL + (n & 3) * 256 + (8 * (g & 1) + 4 * (g >> 1) + t) * 16));
    const v2u tg = *(const LAS v2u*)(uintptr_t)(ldsbase + L_MISC);
    const Tag T{tg.x, tg.y};
    float m = -INFINITY, l = 0.f;
    v4f O[8];
#pragma unroll
    for (int dt = 0; dt < 8; ++dt) O[dt] = v4f{0, 0, 0, 0};
    for (int i0 = 0; i0 < TPW; i0 += 4) {
#pragma unroll
      for (int u = 0; u < 4; ++u) {
        const int i = i0 + u;
        if (i >= 2) {
          if (i + 1 < TPW) asm volatile("s_waitcnt vmcnt(8)" ::: "memory"); else asm volatile("s_waitcnt vmcnt(0)" ::: "memory");
          if (i + 2 < TPW) ISSUE(i + 2, (u + 2) & 3);
        }
        const unsigned so = u * 8192;
        v4u kf[4]; v4s vf[8];
#pragma unroll
        for (int t = 0; t < 4; ++t) kf[t] = *(const LAS v4u*)(uintptr_t)(ka[t] + so);
#pragma unroll
        for (int dt = 0; dt < 8; ++dt) vf[dt] = __builtin_amdgcn_ds_read_tr16_b64_v4i16((LAS v4s*)(uintptr_t)(va[dt] + so));
        v4f S = {0, 0, 0, 0};
#pragma unroll
        for (int t = 0; t < 4; ++t) S = __builtin_amdgcn_mfma_f32_16x16x32_bf16(__builtin_bit_cast(v8bf, kf[t]), qf[t], S, 0, 0, 0);
        const unsigned a01 = pk2(S[0], S[1]), a23 = pk2(S[2], S[3]);
        const unsigned c01 = pk2(lo16(a01) * SCALE, hi16(a01) * SCALE), c23 = pk2(lo16(a23) * SCALE, hi16(a23) * SCALE);
        const float s0 = lo16(c01), s1 = hi16(c01), s2 = lo16(c23), s3 = hi16(c23);
        const float tm = xrow16_max(fmaxf(fmaxf(s0, s1), fmaxf(s2, s3)));
        if (__any(tm > m)) {
          const float mn = fmaxf(m, tm);
          const float alpha = __builtin_amdgcn_exp2f((m - mn) * LOG2E);
          l *= alpha;
#pragma unroll
          for (int dt = 0; dt < 8; ++dt) O[dt] *= alpha;
          m = mn;
        }
        const float p0 = __builtin_amdgcn_exp2f((s0 - m) * LOG2E), p1 = __builtin_amdgcn_exp2f((s1 - m) * LOG2E);
        const float p2 = __builtin_amdgcn_exp2f((s2 - m) * LOG2E), p3 = __builtin_amdgcn_exp2f((s3 - m) * LOG2E);
        l += (p0 + p1) + (p2 + p3);
        const v2u pp = {pk2(p0, p1), pk2(p2, p3)};
        const v4s pf = __builtin_bit_cast(v4s, pp);
#pragma unroll
        for (int dt = 0; dt < 8; ++dt) O[dt] = __builtin_amdgcn_mfma_f32_16x16x16bf16_1k(vf[dt], pf, O[dt], 0, 0, 0);
        asm volatile("s_waitcnt lgkmcnt(0)" ::: "memory");
      }
    }
    l = xrow16_sum(l);
    if (n < 4) {
      const unsigned r = ring + n * 528;
#pragma unroll
      for (int dt = 0; dt < 8; ++dt) *(LAS v4f*)(uintptr_t)(r + 16 + (16 * dt + 4 * g) * 4) = O[dt];
      if (g == 0) *(LAS v2f*)(uintptr_t)r = v2f{m, l};
    }
    BARRIER();
    {
      const int h = wave;
      v2f ml[4], ov[4];
#pragma unroll
      for (int w = 0; w < 4; ++w) { const unsigned r = ldsbase + L_RING + w * (RSLOT * 8192) + h * 528; ml[w] = *(const LAS v2f*)(uintptr_t)r; ov[w] = *(const LAS v2f*)(uintptr_t)(r + 16 + lane * 8); }
      const float M = fmaxf(fmaxf(ml[0].x, ml[1].x), fmaxf(ml[2].x, ml[3].x));
      float L = 0, o0 = 0, o1 = 0;
#pragma unroll
      for (int w = 0; w < 4; ++w) { const float wt = __builtin_amdgcn_exp2f((ml[w].x - M) * LOG2E); L = fmaf(wt, ml[w].y, L); o0 = fmaf(wt, ov[w].x, o0); o1 = fmaf(wt, ov[w].y, o1); }
      const unsigned rec = W_IN1 + ((grp * 16 + 4 * h + g) * 16 + sp) * 320;
      BSTORE16(gran2(o0, o1, T), rec + n * 16);
      if (n == 0) BSTORE16(gran2(M, L, T), rec + 256);
    }
    return;
  }

  const int hw = wave - SW;
  if (hw == 0) {
    const int sig = (wg & 7) * 32 + (wg >> 3);
    v4u x0, x1; v2u ep;
    { const u16* xp = x + lane * 8; const unsigned char* e = ws + W_EPOCH;
      asm volatile("global_load_dwordx4 %0, %3, off\n\tglobal_load_dwordx4 %1, %3, off offset:1024\n\tglobal_load_dwordx2 %2, %4, off sc1" : "=&v"(x0), "=&v"(x1), "=&v"(ep) : "v"(xp), "v"(e) : "memory"); }
    { const char* src = (const char*)wq + (size_t)(lane >> 1) * 8192 + sig * 32 + (lane & 1) * 16;
#pragma unroll
      for (int i = 0; i < 16; ++i) dma1(src + (size_t)i * (32 * 8192), ldsbase + L_STAGE + i * 1024); }
    asm volatile("s_waitcnt vmcnt(16)" : "+v"(x0), "+v"(x1), "+v"(ep) :: "memory");
    const u64 tv = (((u64)ep.y << 32) | ep.x) + 1;
    const Tag T{(unsigned)(tv & 0xff) << 24, (unsigned)(tv >> 8)};
    {
      float v[2][8];
#pragma unroll
      for (int j = 0; j < 4; ++j) { v[0][2 * j] = lo16(x0[j]); v[0][2 * j + 1] = hi16(x0[j]); v[1][2 * j] = lo16(x1[j]); v[1][2 * j + 1] = hi16(x1[j]); }
#pragma unroll
      for (int bb = 0; bb < 2; ++bb) {
        float s = 0;
#pragma unroll
        for (int j = 0; j < 8; ++j) s += v[bb][j];
        const float mean = wave_sum(s) * (1.f / DM);
        float q = 0;
#pragma unroll
        for (int j = 0; j < 8; ++j) { const float d = v[bb][j] - mean; q = fmaf(d, d, q); }
        const float var = wave_sum(q) * (1.f / DM);
        const float mb = rbf(mean);
        const float den = rbf(__builtin_sqrtf(rbf(rbf(var) + 1e-5f)));
        const float inv = 1.f / den;
        v4u o;
#pragma unroll
        for (int j = 0; j < 4; ++j) o[j] = pk2(rbf(v[bb][2 * j] - mb) * inv, rbf(v[bb][2 * j + 1] - mb) * inv);
        *(LAS v4u*)(uintptr_t)(ldsbase + L_XN + bb * 1024 + lane * 16) = o;
      }
    }
    asm volatile("s_waitcnt vmcnt(0) lgkmcnt(0)" ::: "memory");
    const v4f q = proj16(ldsbase, lane);
    if (n < 2) {
      const unsigned a = W_QG + (n * 4096 + 16 * sig + 4 * g) * 8;
      BSTORE16(gran2(rbf(q[0]), rbf(q[1]), T), a);
      BSTORE16(gran2(rbf(q[2]), rbf(q[3]), T), a + 16);
    }
    {
      const unsigned a = W_QG + (b * 4096 + kvh * 512) * 8 + lane * 16;
      v4u v[4];
      for (unsigned spin = 0; spin < SPIN_CAP; ++spin) {
        MEMFENCE();
#pragma unroll
        for (int i = 0; i < 4; ++i) v[i] = BLOAD16(a + i * 1024);
        const bool ok = gok2(v[0], T) & gok2(v[1], T) & gok2(v[2], T) & gok2(v[3], T);
        if (__all(ok)) break;
        __builtin_amdgcn_s_sleep(2);
      }
#pragma unroll
      for (int i = 0; i < 4; ++i) *(LAS unsigned*)(uintptr_t)(ldsbase + L_QL + i * 256 + lane * 4) = ((v[i].x >> 8) & 0xffffu) | ((v[i].z << 8) & 0xffff0000u);
      if (lane == 0) *(LAS v2u*)(uintptr_t)(ldsbase + L_MISC) = v2u{T.lo, T.hi};
    }
  }
  BARRIER();
  const v2u tg = *(const LAS v2u*)(uintptr_t)(ldsbase + L_MISC);
  const Tag T{tg.x, tg.y};
  float gate0 = 0.f, gate1 = 0.f, xr0 = 0.f, xr1 = 0.f;
  if (hw == 1) {
    if ((wg >> 3) < 28) {
      const int phi = (wg & 7) * 28 + (wg >> 3);
      const char* src = (const char*)((lane & 1) ? w3 : w1) + (size_t)(lane >> 1) * (FF * 2) + phi * 16;
#pragma unroll
      for (int i = 0; i < 16; ++i) dma1(src + (size_t)i * (32 * FF * 2), ldsbase + L_STAGE + i * 1024);
      asm volatile("s_waitcnt vmcnt(0)" ::: "memory");
      const v4f r = proj16(ldsbase, lane);
      float hv[4];
#pragma unroll
      for (int i = 0; i < 4; ++i) {
        auto s = __builtin_amdgcn_permlane32_swap(__float_as_uint(r[i]), __float_as_uint(r[i]), false, false);
        const float gt = rbf(__uint_as_float(s[0])), up = rbf(__uint_as_float(s[1]));
        hv[i] = rbf(rbf(silu_f(gt)) * up);
      }
      if (n < 2 && g < 2) {
        const unsigned a = W_HG + (n * FF + 8 * phi + 4 * g) * 8;
        BSTORE16(gran2(hv[0], hv[1], T), a);
        BSTORE16(gran2(hv[2], hv[3], T), a + 16);
      }
    }
  } else if (hw == 2) {
    v4u w[7];
#pragma unroll
    for (int r = 0; r < 7; ++r) w[r] = __builtin_nontemporal_load((const GAS v4u*)(w2 + (size_t)(wg * 7 + r) * DM) + lane);
    const int t = lane < 14 ? lane : 0;
    const unsigned a = W_HG + ((t / 7) * FF + wg * 7 + t % 7) * 8;
    v2u hv;
    for (unsigned spin = 0; spin < SPIN_CAP; ++spin) {
      MEMFENCE();
      hv = BLOAD8(a);
      const bool ok = (hv.y == T.hi) & ((hv.x ^ T.lo) < 0x1000000u);
      if (__all(ok)) break;
      __builtin_amdgcn_s_sleep(8);
    }
    const unsigned hf = hv.x << 8;
    float z[2][8];
#pragma unroll
    for (int c = 0; c < 8; ++c) z[0][c] = z[1][c] = 0.f;
#pragma unroll
    for (int r = 0; r < 7; ++r) {
      const float h0 = __uint_as_float(__builtin_amdgcn_readlane(hf, r)), h1 = __uint_as_float(__builtin_amdgcn_readlane(hf, 7 + r));
#pragma unroll
      for (int j = 0; j < 4; ++j) {
        const float wl = lo16(w[r][j]), wh = hi16(w[r][j]);
        z[0][2 * j] = fmaf(h0, wl, z[0][2 * j]); z[0][2 * j + 1] = fmaf(h0, wh, z[0][2 * j + 1]);
        z[1][2 * j] = fmaf(h1, wl, z[1][2 * j]); z[1][2 * j + 1] = fmaf(h1, wh, z[1][2 * j + 1]);
      }
    }
#pragma unroll
    for (int bb = 0; bb < 2; ++bb)
#pragma unroll
      for (int hf2 = 0; hf2 < 2; ++hf2) {
        const unsigned a2 = W_INZ + ((bb * 128 + 2 * lane + hf2) * 256 + wg) * 32;
        BSTORE16(gran2(z[bb][4 * hf2], z[bb][4 * hf2 + 1], T), a2);
        BSTORE16(gran2(z[bb][4 * hf2 + 2], z[bb][4 * hf2 + 3], T), a2 + 16);
      }
  } else if (hw == 3) {
    if (wg == 0) {
      for (unsigned spin = 0; spin < SPIN_CAP; ++spin) {
        MEMFENCE();
        bool ok = true;
#pragma unroll
        for (int i = 0; i < 4; ++i) { const v2u v = BLOAD8(W_QG + (i * 64 + lane) * 128); ok &= (v.y == T.hi) & ((v.x ^ T.lo) < 0x1000000u); }
        if (__all(ok)) break;
        __builtin_amdgcn_s_sleep(8);
      }
      if (lane == 0) {
        const v2u e = BLOAD8(W_EPOCH);
        const u64 tv = (((u64)e.y << 32) | e.x) + 1;
        __builtin_amdgcn_raw_buffer_store_b64(v2u{(unsigned)tv, (unsigned)(tv >> 32)}, rs, W_EPOCH, 0, 16);
      }
    }
    const unsigned xv = *(const GAS unsigned*)(x + wg * 4 + (lane & 1) * 2);
    xr0 = lo16(xv); xr1 = hi16(xv);
    const unsigned a = W_INZ + wg * 8192 + lane * 16;
    v4u v[8];
    for (unsigned spin = 0; spin < SPIN_CAP; ++spin) {
      __builtin_amdgcn_s_sleep(32);
      MEMFENCE();
      bool ok = true;
#pragma unroll
      for (int i = 0; i < 8; ++i) v[i] = BLOAD16(a + i * 1024);
#pragma unroll
      for (int i = 0; i < 8; ++i) ok &= gok2(v[i], T);
      if (__all(ok)) break;
    }
    float z0 = 0, z1 = 0;
#pragma unroll
    for (int i = 0; i < 8; ++i) { z0 += gval(v[i].x); z1 += gval(v[i].z); }
    z0 = parity_sum(z0); z1 = parity_sum(z1);
    gate0 = rbf(silu_f(rbf(z0))); gate1 = rbf(silu_f(rbf(z1)));
  }
  unsigned wp[4][8];
  {
    const int row0 = (kvh * 4 + (sp >> 2)) * 128 + (sp & 3) * 32 + 8 * g;
    v4u w[8];
#pragma unroll
    for (int i = 0; i < 8; ++i) w[i] = __builtin_nontemporal_load((const GAS v4u*)(wo + (size_t)(row0 + i) * DM + 128 * hw + 8 * n));
#pragma unroll
    for (int k = 0; k < 4; ++k)
#pragma unroll
      for (int j = 0; j < 4; ++j) { wp[k][2 * j] = __builtin_amdgcn_perm(w[2 * k + 1][j], w[2 * k][j], 0x05040100u); wp[k][2 * j + 1] = __builtin_amdgcn_perm(w[2 * k + 1][j], w[2 * k][j], 0x07060302u); }
#pragma unroll
    for (int k = 0; k < 4; ++k)
#pragma unroll
      for (int j = 0; j < 8; ++j) asm volatile("" : "+v"(wp[k][j]));
  }
  BARRIER();
  {
    const unsigned a = W_IN1 + (wg * 16 + n) * 320;
    v4u v[4], ml;
    for (unsigned spin = 0; spin < SPIN_CAP; ++spin) {
      MEMFENCE();
#pragma unroll
      for (int i = 0; i < 4; ++i) v[i] = BLOAD16(a + g * 64 + i * 16);
      ml = BLOAD16(a + 256);
      const bool ok = gok2(v[0], T) & gok2(v[1], T) & gok2(v[2], T) & gok2(v[3], T) & gok2(ml, T);
      if (__all(ok)) break;
      __builtin_amdgcn_s_sleep(1);
    }
    const float mp = gval(ml.x), lp = gval(ml.z);
    const float M = row_max(mp);
    const float wt = __builtin_amdgcn_exp2f((mp - M) * LOG2E);
    const float invL = __builtin_amdgcn_rcpf(row_sum(wt * lp));
    unsigned op[4];
#pragma unroll
    for (int i = 0; i < 4; ++i) op[i] = pk2(row_sum(wt * gval(v[i].x)) * invL, row_sum(wt * gval(v[i].z)) * invL);
    float y[8];
#pragma unroll
    for (int c = 0; c < 8; ++c) { float acc = 0.f;
#pragma unroll
      for (int k = 0; k < 4; ++k) acc = dot2(wp[k][c], op[k], acc);
      y[c] = acc; }
    float t4[4], u2[2];
#pragma unroll
    for (int j = 0; j < 4; ++j) { auto s = __builtin_amdgcn_permlane32_swap(__float_as_uint(y[j]), __float_as_uint(y[j + 4]), false, false); t4[j] = __uint_as_float(s[0]) + __uint_as_float(s[1]); }
#pragma unroll
    for (int j = 0; j < 2; ++j) { auto s = __builtin_amdgcn_permlane16_swap(__float_as_uint(t4[j]), __float_as_uint(t4[j + 2]), false, false); u2[j] = __uint_as_float(s[0]) + __uint_as_float(s[1]); }
    const unsigned a2 = W_INY + ((b * 128 + 32 * hw + 2 * n + (g >> 1)) * 128 + (wg & 127)) * 32 + (g & 1) * 16;
    BSTORE16(gran2(u2[0], u2[1], T), a2);
  }
  if (hw == 3) {
    const unsigned a = W_INY + wg * 4096 + lane * 16;
    v4u v[4];
    for (unsigned spin = 0; spin < SPIN_CAP; ++spin) {
      MEMFENCE();
#pragma unroll
      for (int i = 0; i < 4; ++i) v[i] = BLOAD16(a + i * 1024);
      const bool ok = gok2(v[0], T) & gok2(v[1], T) & gok2(v[2], T) & gok2(v[3], T);
      if (__all(ok)) break;
      __builtin_amdgcn_s_sleep(1);
    }
    float s0 = (gval(v[0].x) + gval(v[1].x)) + (gval(v[2].x) + gval(v[3].x));
    float s1 = (gval(v[0].z) + gval(v[1].z)) + (gval(v[2].z) + gval(v[3].z));
    s0 = parity_sum(s0); s1 = parity_sum(s1);
    if (lane < 2) *(GAS unsigned*)(out + wg * 4 + lane * 2) = pk2(xr0 + rbf(rbf(s0) * gate0), xr1 + rbf(rbf(s1) * gate1));
  }
}

extern "C" void kernel_launch(void* const* d_in, const int* in_sizes, int n_in, void* d_out, int out_size, void* d_ws, size_t ws_size, hipStream_t stream) {
  (void)hipFuncSetAttribute((const void*)k_fused, hipFuncAttributeMaxDynamicSharedMemorySize, LDS_BYTES);
  k_fused<<<NWG, 512, LDS_BYTES, stream>>>((const u16*)d_in[0], (const u16*)d_in[1], (const u16*)d_in[2], (const u16*)d_in[3], (const u16*)d_in[4],
                                           (const u16*)d_in[6], (const u16*)d_in[7], (const u16*)d_in[8], (u16*)d_out, (unsigned char*)d_ws);
}
```

```cpp
#include <hip/hip_runtime.h>
#include <hip/hip_bf16.h>
#include <stdint.h>

#define GAS __attribute__((address_space(1)))
#define LAS __attribute__((address_space(3)))
typedef unsigned v4u __attribute__((ext_vector_type(4)));
typedef unsigned v2u __attribute__((ext_vector_type(2)));
typedef float v4f __attribute__((ext_vector_type(4)));
typedef float v2f __attribute__((ext_vector_type(2)));
typedef __bf16 v8bf __attribute__((ext_vector_type(8)));
typedef __bf16 v2bf __attribute__((ext_vector_type(2)));
typedef short v4s __attribute__((ext_vector_type(4)));
typedef short v8s __attribute__((ext_vector_type(8)));
typedef unsigned short u16;
typedef unsigned long long u64;

constexpr int DM = 512, SK = 32768, FF = 1792;
constexpr int NWG = 256, NSPLIT = 16;
constexpr float SCALE = 0.088388347648318440f, LOG2E = 1.4426950408889634f;
constexpr int SW = 4, RSLOT = 4;
#define NSTAT 24
#define DEPTH 2
#define NO_MLP 0
#define NO_STREAM 0
#define NO_SWZ 0
#define POLL_SLEEP 0
#define WO_WHEN 2
constexpr int NT_CLASS = SK / 16 / SW, DYN0 = NSTAT * NSPLIT;

constexpr int L_RING = 0, L_STAGE = SW * RSLOT * 8192, L_XN = L_STAGE + 16384, L_QL = L_XN + 2048, L_MISC = L_QL + 1024, L_H = L_MISC + 256, L_OP = L_H + 256, L_QP = L_OP + 256,
              LDS_BYTES = L_QP + 512;
constexpr unsigned W_EPOCH = 0, W_CTR = 4096, W_QG = W_CTR + 64 * 256, W_HG = W_QG + 2 * 4096 * 8, W_IN1 = W_HG + 16 * FF * 8, W_INY = W_IN1 + NWG * 16 * 320, W_INZ = W_INY + NWG * 4096,
                   W_END = W_INZ + NWG * 2048;
constexpr unsigned SPIN_CAP = 1u << 16;

__device__ __forceinline__ float bf2f(unsigned v) { return __uint_as_float(v << 16); }
__device__ __forceinline__ u16 f2bf(float f) { __bf16 b = (__bf16)f; return __builtin_bit_cast(u16, b); }
__device__ __forceinline__ float rbf(float f) { return bf2f(f2bf(f)); }
__device__ __forceinline__ unsigned pk2(float lo, float hi) { v2bf p = {(__bf16)lo, (__bf16)hi}; return __builtin_bit_cast(unsigned, p); }
__device__ __forceinline__ float lo16(unsigned v) { return __uint_as_float(v << 16); }
__device__ __forceinline__ float hi16(unsigned v) { return __uint_as_float(v & 0xffff0000u); }
template <int CTRL> __device__ __forceinline__ float dpp(float x) {
  return __builtin_bit_cast(float, __builtin_amdgcn_mov_dpp(__builtin_bit_cast(int, x), CTRL, 0xf, 0xf, true));
}
constexpr int XOR1 = 0xB1, XOR2 = 0x4E, HMIR = 0x141, MIR = 0x140, ROR2 = 0x122, ROR4 = 0x124, ROR8 = 0x128;
__device__ __forceinline__ float swap32_add(float a, float b) { auto s = __builtin_amdgcn_permlane32_swap(__float_as_uint(a), __float_as_uint(b), false, false); return __uint_as_float(s[0]) + __uint_as_float(s[1]); }
__device__ __forceinline__ float swap16_add(float a, float b) { auto s = __builtin_amdgcn_permlane16_swap(__float_as_uint(a), __float_as_uint(b), false, false); return __uint_as_float(s[0]) + __uint_as_float(s[1]); }
__device__ __forceinline__ float xrow16_max(float x) {
  auto s = __builtin_amdgcn_permlane16_swap(__float_as_uint(x), __float_as_uint(x), false, false);
  x = fmaxf(__uint_as_float(s[0]), __uint_as_float(s[1]));
  auto t = __builtin_amdgcn_permlane32_swap(__float_as_uint(x), __float_as_uint(x), false, false);
  return fmaxf(__uint_as_float(t[0]), __uint_as_float(t[1]));
}
__device__ __forceinline__ float xrow16_sum(float x) { x = swap16_add(x, x); return swap32_add(x, x); }
__device__ __forceinline__ float row_sum(float x) { x += dpp<XOR1>(x); x += dpp<XOR2>(x); x += dpp<HMIR>(x); x += dpp<MIR>(x); return x; }
__device__ __forceinline__ float row_max(float x) { x = fmaxf(x, dpp<XOR1>(x)); x = fmaxf(x, dpp<XOR2>(x)); x = fmaxf(x, dpp<HMIR>(x)); x = fmaxf(x, dpp<MIR>(x)); return x; }
__device__ __forceinline__ float wave_sum(float x) { return xrow16_sum(row_sum(x)); }
__device__ __forceinline__ float parity_sum(float x) { x += dpp<ROR2>(x); x += dpp<ROR4>(x); x += dpp<ROR8>(x); return xrow16_sum(x); }
__device__ __forceinline__ float silu_f(float x) { return x * __builtin_amdgcn_rcpf(1.f + __builtin_amdgcn_exp2f(-x * LOG2E)); }
__device__ __forceinline__ float dot2(unsigned a, unsigned b, float c) { return __builtin_amdgcn_fdot2_f32_bf16(__builtin_bit_cast(v2bf, a), __builtin_bit_cast(v2bf, b), c, false); }

#define STAMP(i) do {} while (0)
#define STAMP_DECL do {} while (0)
#define STAMP_FLUSH() do {} while (0)

struct Tag { unsigned lo, hi; };
__device__ __forceinline__ v4u gran2(float a, float b, Tag t) { return v4u{(__float_as_uint(a) >> 8) | t.lo, t.hi, (__float_as_uint(b) >> 8) | t.lo, t.hi}; }
__device__ __forceinline__ bool gok2(v4u v, Tag t) { return (v.y == t.hi) & (v.w == t.hi) & (((v.x ^ t.lo) | (v.z ^ t.lo)) < 0x1000000u); }
__device__ __forceinline__ bool gok1(v2u v, Tag t) { return (v.y == t.hi) & ((v.x ^ t.lo) < 0x1000000u); }
__device__ __forceinline__ float gval(unsigned lo) { return __uint_as_float(lo << 8); }
#define BLOAD16(off) __builtin_amdgcn_raw_buffer_load_b128(rs, (off), 0, 16)
#define BLOAD8(off) __builtin_amdgcn_raw_buffer_load_b64(rs, (off), 0, 16)
#define BSTORE16(v, off) __builtin_amdgcn_raw_buffer_store_b128((v), rs, (off), 0, 16)
#define MEMFENCE() asm volatile("" ::: "memory")
#define LGKM0() asm volatile("s_waitcnt lgkmcnt(0)" ::: "memory")
#define BARRIER() do { LGKM0(); __builtin_amdgcn_s_barrier(); MEMFENCE(); } while (0)
#define LDSP(T, a) ((LAS T*)(uintptr_t)(a))

#define DMA_TILE(kb, vb, ldsaddr)                                                              \
  asm volatile("s_mov_b32 %0, m0\n\t"                                                           \
               "s_mov_b32 m0, %7\n\t"                                                           \
               "s_nop 0\n\t"                                                                    \
               "global_load_lds_dwordx4 %1, %5 nt\n\t"                                          \
               "global_load_lds_dwordx4 %2, %5 offset:1024 nt\n\t"                              \
               "global_load_lds_dwordx4 %1, %5 offset:2048 nt\n\t"                              \
               "global_load_lds_dwordx4 %2, %5 offset:3072 nt\n\t"                              \
               "s_add_u32 m0, m0, 0x1000\n\t"                                                   \
               "s_nop 0\n\t"                                                                    \
               "global_load_lds_dwordx4 %3, %6 nt\n\t"                                          \
               "global_load_lds_dwordx4 %4, %6 offset:1024 nt\n\t"                              \
               "global_load_lds_dwordx4 %3, %6 offset:2048 nt\n\t"                              \
               "global_load_lds_dwordx4 %4, %6 offset:3072 nt\n\t"                              \
               "s_mov_b32 m0, %0"                                                               \
               : "=&s"(m0keep) : "v"(koe), "v"(koo), "v"(voe), "v"(voo), "s"(kb), "s"(vb), "s"(ldsaddr) : "memory", "scc")

__device__ __forceinline__ void dma1(const void* gsrc, unsigned lds_dst) {
  unsigned keep;
  asm volatile("s_mov_b32 %0, m0\n\ts_mov_b32 m0, %2\n\ts_nop 0\n\tglobal_load_lds_dwordx4 %1, off nt\n\ts_mov_b32 m0, %0" : "=&s"(keep) : "v"(gsrc), "s"(lds_dst) : "memory");
}
__device__ __forceinline__ v4f mfma_tr(unsigned a_lo, unsigned a_hi, v8s bb, v4f acc) {
  const v4s a0 = __builtin_amdgcn_ds_read_tr16_b64_v4i16(LDSP(v4s, a_lo));
  const v4s a1 = __builtin_amdgcn_ds_read_tr16_b64_v4i16(LDSP(v4s, a_hi));
  const v8s a = __builtin_shufflevector(a0, a1, 0, 1, 2, 3, 4, 5, 6, 7);
  return __builtin_amdgcn_mfma_f32_16x16x32_bf16(__builtin_bit_cast(v8bf, a), __builtin_bit_cast(v8bf, bb), acc, 0, 0, 0);
}

__global__ void __launch_bounds__(512, 2) k_fused(const u16* __restrict__ x, const u16* __restrict__ Kc, const u16* __restrict__ Vc, const u16* __restrict__ wq,
                                                  const u16* __restrict__ wo, const u16* __restrict__ w1, const u16* __restrict__ w3, const u16* __restrict__ w2,
                                                  u16* __restrict__ out, unsigned char* __restrict__ ws) {
  extern __shared__ __attribute__((aligned(16))) unsigned char lds[];
  const int tid = threadIdx.x, lane = tid & 63, wave = __builtin_amdgcn_readfirstlane(tid >> 6);
  const int wg = blockIdx.x, grp = wg >> 4, sp = wg & 15, b = grp >> 3, kvh = grp & 7;
  const unsigned ldsbase = (unsigned)(uintptr_t)(LAS unsigned char*)lds;
  const __amdgpu_buffer_rsrc_t rs = __builtin_amdgcn_make_buffer_rsrc(ws, 0, W_END, 0x00020000);
  const int n = lane & 15, g = lane >> 4;
  STAMP_DECL; STAMP(0);

  if (wave < SW) {
    const unsigned ring = ldsbase + L_RING + wave * (RSLOT * 8192);
    const unsigned r4 = lane >> 4, pos = lane & 15;
    const unsigned koe = NO_SWZ ? lane * 16 : r4 * 256 + ((pos & 8) | ((pos & 7) ^ r4)) * 16;
    const unsigned koo = NO_SWZ ? lane * 16 : r4 * 256 + ((pos & 8) | ((pos & 7) ^ (4 + r4))) * 16;
    const unsigned voe = NO_SWZ ? lane * 16 : r4 * 256 + (pos ^ (r4 << 1)) * 16;
    const unsigned voo = NO_SWZ ? lane * 16 : r4 * 256 + (pos ^ ((4 + r4) << 1)) * 16;
    const size_t gbase = (size_t)grp * SK * 256 + (size_t)wave * 4096;
    const int t0s = sp * NSTAT;
    const GAS unsigned* ctr = (const GAS unsigned*)(ws + W_CTR + (grp * SW + wave) * 256);
    const unsigned one = 1;
    const char* kptr = (const char*)Kc + gbase;
    const char* vptr = (const char*)Vc + gbase;
    unsigned m0keep;
#define ISSUE(t, slot) do { if (NO_STREAM) break; const char* k0_ = kptr + (size_t)(t) * (SW * 4096); const char* v0_ = vptr + (size_t)(t) * (SW * 4096); const unsigned l_ = ring + (slot) * 8192; DMA_TILE(k0_, v0_, l_); } while (0)
#define TICKET(par) do { if (par) asm volatile("s_mov_b64 exec, 1\n\tglobal_atomic_add v251, %0, %1, off sc0\n\ts_mov_b64 exec, -1" :: "v"(ctr), "v"(one) : "memory", "v251"); \
                        else asm volatile("s_mov_b64 exec, 1\n\tglobal_atomic_add v250, %0, %1, off sc0\n\ts_mov_b64 exec, -1" :: "v"(ctr), "v"(one) : "memory", "v250"); } while (0)
    __builtin_amdgcn_s_barrier();
    ISSUE(t0s, 0); ISSUE(t0s + 1, 1);
    if (DEPTH == 3) { ISSUE(t0s + 2, 2); asm volatile("s_waitcnt vmcnt(16)" ::: "memory"); STAMP(1); ISSUE(t0s + 3, 3); }
    else {
      asm volatile("s_waitcnt vmcnt(8)" ::: "memory");
      STAMP(1);
      ISSUE(t0s + 2, 2);
      asm volatile("s_waitcnt vmcnt(8)" ::: "memory");
      ISSUE(t0s + 3, 3);
    }
    unsigned ka[4], va[8];
#pragma unroll
    for (int t = 0; t < 4; ++t) ka[t] = ring + n * 256 + ((8 * (g & 1)) | ((4 * (g >> 1) + t) ^ (n & 7))) * 16;
    { const unsigned q4 = (lane >> 2) & 3, p = lane & 3, row = 4 * g + q4;
#pragma unroll
      for (int dt = 0; dt < 8; ++dt) va[dt] = ring + 4096 + row * 256 + ((dt ^ (row & 7)) << 5) + p * 8; }
    STAMP(2);
    BARRIER();
    STAMP(3);
    v8bf qf[4];
#pragma unroll
    for (int t = 0; t < 4; ++t) qf[t] = __builtin_bit_cast(v8bf, *LDSP(const v4u, ldsbase + L_QL + (n & 3) * 256 + (8 * (g & 1) + 4 * (g >> 1) + t) * 16));
    const v2u tg = *LDSP(const v2u, ldsbase + L_MISC);
    const Tag T{tg.x, tg.y};
    float m = -INFINITY, l = 0.f;
    v4f O[8];
#pragma unroll
    for (int dt = 0; dt < 8; ++dt) O[dt] = v4f{0, 0, 0, 0};
    int i = 0, rem = NO_STREAM ? 0 : 3; bool live = !NO_STREAM;
    for (;;) {
      bool done = false;
#pragma unroll
      for (int u = 0; u < 4; ++u) {
        if (done) break;
        if (i >= RSLOT - DEPTH) {
          if (rem >= 2 && DEPTH == 3) asm volatile("s_waitcnt vmcnt(16)" ::: "memory");
          else if (rem >= 1) asm volatile("s_waitcnt vmcnt(8)" ::: "memory");
          else asm volatile("s_waitcnt vmcnt(0)" ::: "memory");
          if (live) {
            int t = t0s + i + DEPTH;
            if (i + DEPTH >= NSTAT) {
              int tk = NT_CLASS;
              if (DYN0 < NT_CLASS) { if ((u - (DEPTH - 1)) & 1) asm volatile("v_readfirstlane_b32 %0, v251" : "=s"(tk) :: "memory"); else asm volatile("v_readfirstlane_b32 %0, v250" : "=s"(tk) :: "memory"); }
              t = DYN0 + tk;
              live = (unsigned)tk < (unsigned)(NT_CLASS - DYN0);
            }
            if (live) {
              if (i + 2 * DEPTH - 1 >= NSTAT && DYN0 < NT_CLASS) TICKET(u & 1);
              ISSUE(t, (u + DEPTH) & 3);
              ++rem;
            }
          }
        }
        const unsigned so = u * 8192;
        v4u kf[4]; v4s vf[8];
#pragma unroll
        for (int t = 0; t < 4; ++t) kf[t] = *LDSP(const v4u, ka[t] + so);
#pragma unroll
        for (int dt = 0; dt < 8; ++dt) vf[dt] = __builtin_amdgcn_ds_read_tr16_b64_v4i16(LDSP(v4s, va[dt] + so));
        v4f S = {0, 0, 0, 0};
#pragma unroll
        for (int t = 0; t < 4; ++t) S = __builtin_amdgcn_mfma_f32_16x16x32_bf16(__builtin_bit_cast(v8bf, kf[t]), qf[t], S, 0, 0, 0);
        const unsigned a01 = pk2(S[0], S[1]), a23 = pk2(S[2], S[3]);
        const unsigned c01 = pk2(lo16(a01) * SCALE, hi16(a01) * SCALE), c23 = pk2(lo16(a23) * SCALE, hi16(a23) * SCALE);
        const float s0 = lo16(c01), s1 = hi16(c01), s2 = lo16(c23), s3 = hi16(c23);
        const float tm = xrow16_max(fmaxf(fmaxf(s0, s1), fmaxf(s2, s3)));
        if (__any(tm > m)) {
          const float mn = fmaxf(m, tm);
          const float alpha = __builtin_amdgcn_exp2f((m - mn) * LOG2E);
          l *= alpha;
#pragma unroll
          for (int dt = 0; dt < 8; ++dt) O[dt] *= alpha;
          m = mn;
        }
        const float p0 = __builtin_amdgcn_exp2f((s0 - m) * LOG2E), p1 = __builtin_amdgcn_exp2f((s1 - m) * LOG2E);
        const float p2 = __builtin_amdgcn_exp2f((s2 - m) * LOG2E), p3 = __builtin_amdgcn_exp2f((s3 - m) * LOG2E);
        l += (p0 + p1) + (p2 + p3);
        const v2u pp = {pk2(p0, p1), pk2(p2, p3)};
        const v4s pf = __builtin_bit_cast(v4s, pp);
#pragma unroll
        for (int dt = 0; dt < 8; ++dt) O[dt] = __builtin_amdgcn_mfma_f32_16x16x16bf16_1k(vf[dt], pf, O[dt], 0, 0, 0);
        LGKM0();
        if (rem == 0) done = true;
        --rem; ++i;
      }
      if (done) break;
    }
    STAMP(4);
    l = xrow16_sum(l);
    if (n < 4) {
      const unsigned r = ring + n * 528;
#pragma unroll
      for (int dt = 0; dt < 8; ++dt) *LDSP(v4f, r + 16 + (16 * dt + 4 * g) * 4) = O[dt];
      if (g == 0) *LDSP(v2f, r) = v2f{m, l};
    }
    BARRIER();
    STAMP(5);
    {
      const int h = wave;
      v2f ml[4], ov[4];
#pragma unroll
      for (int w = 0; w < 4; ++w) { const unsigned r = ldsbase + L_RING + w * (RSLOT * 8192) + h * 528; ml[w] = *LDSP(const v2f, r); ov[w] = *LDSP(const v2f, r + 16 + lane * 8); }
      const float M = fmaxf(fmaxf(ml[0].x, ml[1].x), fmaxf(ml[2].x, ml[3].x));
      float L = 0, o0 = 0, o1 = 0;
#pragma unroll
      for (int w = 0; w < 4; ++w) { const float wt = __builtin_amdgcn_exp2f((ml[w].x - M) * LOG2E); L = fmaf(wt, ml[w].y, L); o0 = fmaf(wt, ov[w].x, o0); o1 = fmaf(wt, ov[w].y, o1); }
      const unsigned rec = W_IN1 + ((grp * 16 + 4 * h + g) * 16 + sp) * 320;
      BSTORE16(gran2(o0, o1, T), rec + n * 16);
      if (n == 0) BSTORE16(gran2(M, L, T), rec + 256);
    }
    STAMP(6); STAMP_FLUSH();
    return;
  }

  const int hw = wave - SW;
  Tag T;
  unsigned wp[4][8];
#define LOAD_WO() do { const int row0 = (kvh * 4 + (sp >> 2)) * 128 + (sp & 3) * 32 + 8 * g; v4u w_[8];                                                              \
    _Pragma("unroll") for (int i = 0; i < 8; ++i) w_[i] = __builtin_nontemporal_load((const GAS v4u*)(wo + (size_t)(row0 + i) * DM + 128 * hw + 8 * n));              \
    _Pragma("unroll") for (int k = 0; k < 4; ++k) _Pragma("unroll") for (int j = 0; j < 4; ++j) {                                                                   \
      wp[k][2 * j] = __builtin_amdgcn_perm(w_[2 * k + 1][j], w_[2 * k][j], 0x05040100u); wp[k][2 * j + 1] = __builtin_amdgcn_perm(w_[2 * k + 1][j], w_[2 * k][j], 0x07060302u); } \
    _Pragma("unroll") for (int k = 0; k < 4; ++k) _Pragma("unroll") for (int j = 0; j < 8; ++j) asm volatile("" : "+v"(wp[k][j])); } while (0)
  {
    const int sig = (wg & 7) * 32 + (wg >> 3);
    v4u x0, x1; v2u ep;
    { const u16* xp = x + lane * 8; const unsigned char* e = ws + W_EPOCH;
      asm volatile("global_load_dwordx4 %0, %3, off nt\n\tglobal_load_dwordx4 %1, %3, off offset:1024 nt\n\tglobal_load_dwordx2 %2, %4, off nt" : "=&v"(x0), "=&v"(x1), "=&v"(ep) : "v"(xp), "v"(e) : "memory"); }
    { const char* src = (const char*)wq + (size_t)(128 * hw + (lane >> 1)) * 8192 + sig * 32 + (lane & 1) * 16;
#pragma unroll
      for (int i = 0; i < 4; ++i) dma1(src + (size_t)i * (32 * 8192), ldsbase + L_STAGE + (4 * hw + i) * 1024); }
    if (hw == 0 && lane == 0) { *LDSP(v4u, ldsbase + L_QP) = v4u{0, 0, 0, 0}; *LDSP(unsigned, ldsbase + L_OP + 64) = 0u; }
    BARRIER();
    asm volatile("s_waitcnt vmcnt(4)" : "+v"(x0), "+v"(x1), "+v"(ep) :: "memory");
    STAMP(1);
    if (hw == 0 && wg < 16 && lane < SW)
      __builtin_amdgcn_raw_buffer_store_b32(0u, rs, W_CTR + (((wg >> 3) * 8 + (wg & 7)) * SW + lane) * 256, 0, 16);
    const u64 tv = (((u64)ep.y << 32) | ep.x) + 1;
    T = Tag{(unsigned)(tv & 0xff) << 24, (unsigned)(tv >> 8)};
    {
      float v[2][8];
#pragma unroll
      for (int j = 0; j < 4; ++j) { v[0][2 * j] = lo16(x0[j]); v[0][2 * j + 1] = hi16(x0[j]); v[1][2 * j] = lo16(x1[j]); v[1][2 * j + 1] = hi16(x1[j]); }
#pragma unroll
      for (int bb = 0; bb < 2; ++bb) {
        const float s = ((v[bb][0] + v[bb][1]) + (v[bb][2] + v[bb][3])) + ((v[bb][4] + v[bb][5]) + (v[bb][6] + v[bb][7]));
        const float mean = wave_sum(s) * (1.f / DM);
        float q = 0;
#pragma unroll
        for (int j = 0; j < 8; ++j) { const float d = v[bb][j] - mean; q = fmaf(d, d, q); }
        const float var = wave_sum(q) * (1.f / DM);
        const float mb = rbf(mean);
        const float den = rbf(__builtin_amdgcn_sqrtf(rbf(rbf(var) + 1e-5f)));
        const float inv = __builtin_amdgcn_rcpf(den);
        v4u o;
#pragma unroll
        for (int j = 0; j < 4; ++j) o[j] = pk2(rbf(v[bb][2 * j] - mb) * inv, rbf(v[bb][2 * j + 1] - mb) * inv);
        const unsigned xa = ldsbase + L_XN + bb * 1024 + (lane >> 2) * 64 + (lane & 1) * 32 + ((lane >> 1) & 1) * 8;
        if (g == hw) { *LDSP(v2u, xa) = v2u{o[0], o[1]}; *LDSP(v2u, xa + 16) = v2u{o[2], o[3]}; }
      }
    }
    asm volatile("s_waitcnt vmcnt(0) lgkmcnt(0)" ::: "memory");
    STAMP(2);
    v4f q;
    {
      const unsigned aaddr = ldsbase + L_STAGE + hw * 4096 + lane * 8, baddr = ldsbase + L_XN + (lane & 1) * 1024 + hw * 256 + g * 16;
      v4f a0 = {0, 0, 0, 0}, a1 = {0, 0, 0, 0};
      a0 = mfma_tr(aaddr, aaddr + 512, *LDSP(const v8s, baddr), a0);
      a1 = mfma_tr(aaddr + 1024, aaddr + 1536, *LDSP(const v8s, baddr + 64), a1);
      a0 = mfma_tr(aaddr + 2048, aaddr + 2560, *LDSP(const v8s, baddr + 128), a0);
      a1 = mfma_tr(aaddr + 3072, aaddr + 3584, *LDSP(const v8s, baddr + 192), a1);
      q = a0 + a1;
    }
    if (hw != 0) {
      if (n < 2) *LDSP(v4f, ldsbase + L_QP + hw * 128 + (g * 2 + n) * 16) = q;
      LGKM0();
      if (lane == 0) *LDSP(volatile unsigned, ldsbase + L_QP + hw * 4) = 1u;
      if (WO_WHEN == 0) LOAD_WO();
    } else {
      for (unsigned spin = 0; spin < (SPIN_CAP << 6); ++spin) {
        const v4u f = *LDSP(volatile v4u, ldsbase + L_QP);
        if (f.y & f.z & f.w) break;
      }
      MEMFENCE();
      const unsigned qa = ldsbase + L_QP + (g * 2 + (n & 1)) * 16;
      q = (q + *LDSP(const v4f, qa + 128)) + (*LDSP(const v4f, qa + 256) + *LDSP(const v4f, qa + 384));
      if (n < 2) {
        const unsigned a = W_QG + (n * 4096 + 16 * sig + 4 * g) * 8;
        BSTORE16(gran2(rbf(q[0]), rbf(q[1]), T), a);
        BSTORE16(gran2(rbf(q[2]), rbf(q[3]), T), a + 16);
      }
      STAMP(3);
      const unsigned a = W_QG + (b * 4096 + kvh * 512) * 8 + lane * 16;
      v4u v[4];
      bool ok[4] = {false, false, false, false};
      for (unsigned spin = 0; spin < SPIN_CAP; ++spin) {
        MEMFENCE();
#pragma unroll
        for (int i = 0; i < 4; ++i) if (!ok[i]) v[i] = BLOAD16(a + i * 1024);
#pragma unroll
        for (int i = 0; i < 4; ++i) ok[i] = gok2(v[i], T);
        if (__all(ok[0] & ok[1] & ok[2] & ok[3])) break;
        __builtin_amdgcn_s_sleep(1);
      }
#pragma unroll
      for (int i = 0; i < 4; ++i) *LDSP(unsigned, ldsbase + L_QL + i * 256 + lane * 4) = ((v[i].x >> 8) & 0xffffu) | ((v[i].z << 8) & 0xffff0000u);
      if (lane == 0) *LDSP(v2u, ldsbase + L_MISC) = v2u{T.lo, T.hi};
    }
  }
  BARRIER();
  STAMP(4);
  if (WO_WHEN == 1 || (WO_WHEN == 0 && hw == 0)) LOAD_WO();
  float gate0 = 0.f, gate1 = 0.f, xr0 = 0.f, xr1 = 0.f;
  if (NO_MLP) {
  } else if (hw == 1) {
    if (wg < 224) {
      const int mat = wg >= 112, rem = wg - 112 * mat, cbk = rem >> 2, rbk = rem & 3;
      const char* src = (const char*)(mat ? w3 : w1) + (size_t)(128 * rbk + (lane >> 3)) * (FF * 2) + cbk * 128 + (lane & 7) * 16;
#pragma unroll
      for (int i = 0; i < 16; ++i) dma1(src + (size_t)i * (8 * FF * 2), ldsbase + L_STAGE + i * 1024);
      asm volatile("s_waitcnt vmcnt(0)" ::: "memory");
      const unsigned aaddr = ldsbase + L_STAGE + (lane >> 2) * 128 + (lane & 3) * 8, baddr = ldsbase + L_XN + (lane & 1) * 1024 + rbk * 256 + g * 16;
      v4f acc[4];
#pragma unroll
      for (int ct = 0; ct < 4; ++ct) acc[ct] = v4f{0, 0, 0, 0};
#pragma unroll
      for (int s = 0; s < 4; ++s) {
        const v8s bb = *LDSP(const v8s, baddr + s * 64);
#pragma unroll
        for (int ct = 0; ct < 4; ++ct) acc[ct] = mfma_tr(aaddr + s * 4096 + ct * 32, aaddr + s * 4096 + 2048 + ct * 32, bb, acc[ct]);
      }
      if (n < 2) {
        const unsigned a = W_HG + (((mat * 2 + n) * 4 + rbk) * FF + 64 * cbk + 4 * g) * 8;
#pragma unroll
        for (int ct = 0; ct < 4; ++ct) { BSTORE16(gran2(acc[ct][0], acc[ct][1], T), a + ct * 128); BSTORE16(gran2(acc[ct][2], acc[ct][3], T), a + ct * 128 + 16); }
      }
    }
  } else if (hw == 2) {
    const int rb = wg >> 2, cb = wg & 3;
    v4u w[7];
#pragma unroll
    for (int r = 0; r < 7; ++r) w[r] = __builtin_nontemporal_load((const GAS v4u*)(w2 + (size_t)(rb * 28 + g * 7 + r) * DM + cb * 128 + n * 8));
    const int t = lane < 56 ? lane : 0, tb = t / 28, tr = t % 28;
    const unsigned a = W_HG + (tb * 4 * FF + rb * 28 + tr) * 8;
    v2u pv[8];
    __builtin_amdgcn_s_sleep(127);
    for (unsigned spin = 0; spin < SPIN_CAP; ++spin) {
      __builtin_amdgcn_s_sleep(64);
      MEMFENCE();
      bool ok = true;
#pragma unroll
      for (int i = 0; i < 8; ++i) pv[i] = BLOAD8(a + ((i >> 2) * 8 + (i & 3)) * FF * 8);
#pragma unroll
      for (int i = 0; i < 8; ++i) ok &= gok1(pv[i], T);
      if (__all(ok)) break;
    }
    const float gt = rbf((gval(pv[0].x) + gval(pv[1].x)) + (gval(pv[2].x) + gval(pv[3].x))), up = rbf((gval(pv[4].x) + gval(pv[5].x)) + (gval(pv[6].x) + gval(pv[7].x)));
    const float hval = rbf(rbf(silu_f(gt)) * up);
    if (lane < 56) *LDSP(float, ldsbase + L_H + (tb * 32 + (tr / 7) * 8 + tr % 7) * 4) = hval;
    LGKM0();
#pragma unroll
    for (int bb = 0; bb < 2; ++bb) {
      const v4f h0 = *LDSP(const v4f, ldsbase + L_H + (bb * 32 + g * 8) * 4), h1 = *LDSP(const v4f, ldsbase + L_H + (bb * 32 + g * 8 + 4) * 4);
      const float hh[7] = {h0[0], h0[1], h0[2], h0[3], h1[0], h1[1], h1[2]};
      float z[8];
#pragma unroll
      for (int c = 0; c < 8; ++c) z[c] = 0.f;
#pragma unroll
      for (int r = 0; r < 7; ++r)
#pragma unroll
        for (int j = 0; j < 4; ++j) { z[2 * j] = fmaf(hh[r], lo16(w[r][j]), z[2 * j]); z[2 * j + 1] = fmaf(hh[r], hi16(w[r][j]), z[2 * j + 1]); }
      float t4[4];
#pragma unroll
      for (int j = 0; j < 4; ++j) t4[j] = swap32_add(z[j], z[j + 4]);
      BSTORE16(gran2(swap16_add(t4[0], t4[2]), swap16_add(t4[1], t4[3]), T), W_INZ + ((bb * 128 + 32 * cb + 2 * n + (g >> 1)) * 64 + rb) * 32 + (g & 1) * 16);
    }
  } else if (hw == 3) {
    if (wg == 0) {
      for (unsigned spin = 0; spin < SPIN_CAP; ++spin) {
        __builtin_amdgcn_s_sleep(32);
        MEMFENCE();
        bool ok = true;
#pragma unroll
        for (int i = 0; i < 4; ++i) ok &= gok1(BLOAD8(W_QG + (i * 64 + lane) * 128), T);
        if (__all(ok)) break;
      }
      if (lane == 0) {
        const v2u e = BLOAD8(W_EPOCH);
        const u64 tv = (((u64)e.y << 32) | e.x) + 1;
        __builtin_amdgcn_raw_buffer_store_b64(v2u{(unsigned)tv, (unsigned)(tv >> 32)}, rs, W_EPOCH, 0, 16);
      }
    }
    const unsigned xv = __builtin_nontemporal_load((const GAS unsigned*)(x + wg * 4 + (lane & 1) * 2));
    xr0 = lo16(xv); xr1 = hi16(xv);
    const unsigned a = W_INZ + wg * 2048 + lane * 16;
    v4u v[2];
    __builtin_amdgcn_s_sleep(127); __builtin_amdgcn_s_sleep(127); __builtin_amdgcn_s_sleep(127);
    for (unsigned spin = 0; spin < SPIN_CAP; ++spin) {
      __builtin_amdgcn_s_sleep(64);
      MEMFENCE();
      v[0] = BLOAD16(a); v[1] = BLOAD16(a + 1024);
      if (__all(gok2(v[0], T) & gok2(v[1], T))) break;
    }
    const float z0 = parity_sum(gval(v[0].x) + gval(v[1].x)), z1 = parity_sum(gval(v[0].z) + gval(v[1].z));
    gate0 = rbf(silu_f(rbf(z0))); gate1 = rbf(silu_f(rbf(z1)));
  }
  if (hw != 0) STAMP(1);
  if (WO_WHEN == 2) LOAD_WO();
  if (hw != 0) STAMP(2);
  BARRIER();
  STAMP(5);
  {
    unsigned op[4];
    if (hw == 0) {
      const unsigned a = W_IN1 + (wg * 16 + n) * 320;
      v4u v[4], ml;
      bool ok = false;
      for (unsigned spin = 0; spin < SPIN_CAP; ++spin) {
        MEMFENCE();
        if (!ok) {
#pragma unroll
          for (int i = 0; i < 4; ++i) v[i] = BLOAD16(a + g * 64 + i * 16);
          ml = BLOAD16(a + 256);
          ok = gok2(v[0], T) & gok2(v[1], T) & gok2(v[2], T) & gok2(v[3], T) & gok2(ml, T);
        }
        if (__all(ok)) break;
        if (POLL_SLEEP) __builtin_amdgcn_s_sleep(POLL_SLEEP);
      }
      STAMP(6);
      const float mp = gval(ml.x), lp = gval(ml.z);
      const float M = row_max(mp);
      const float wt = __builtin_amdgcn_exp2f((mp - M) * LOG2E);
      const float invL = __builtin_amdgcn_rcpf(row_sum(wt * lp));
#pragma unroll
      for (int i = 0; i < 4; ++i) op[i] = pk2(row_sum(wt * gval(v[i].x)) * invL, row_sum(wt * gval(v[i].z)) * invL);
      if (n == 0) *LDSP(v4u, ldsbase + L_OP + g * 16) = v4u{op[0], op[1], op[2], op[3]};
      LGKM0();
      if (lane == 0) *LDSP(volatile unsigned, ldsbase + L_OP + 64) = 1u;
    } else {
      for (unsigned spin = 0; spin < (SPIN_CAP << 8); ++spin) {
        if (*LDSP(volatile unsigned, ldsbase + L_OP + 64)) break;
        __builtin_amdgcn_s_sleep(1);
      }
      MEMFENCE();
      const v4u o4 = *LDSP(const v4u, ldsbase + L_OP + g * 16);
      op[0] = o4.x; op[1] = o4.y; op[2] = o4.z; op[3] = o4.w;
      STAMP(6);
    }
    float y[8];
#pragma unroll
    for (int c = 0; c < 8; ++c) { float acc = 0.f;
#pragma unroll
      for (int k = 0; k < 4; ++k) acc = dot2(wp[k][c], op[k], acc);
      y[c] = acc; }
    float t4[4];
#pragma unroll
    for (int j = 0; j < 4; ++j) t4[j] = swap32_add(y[j], y[j + 4]);
    const unsigned a2 = W_INY + ((b * 128 + 32 * hw + 2 * n + (g >> 1)) * 128 + (wg & 127)) * 32 + (g & 1) * 16;
    BSTORE16(gran2(swap16_add(t4[0], t4[2]), swap16_add(t4[1], t4[3]), T), a2);
  }
  if (hw == 3) {
    const unsigned a = W_INY + wg * 4096 + lane * 16;
    v4u v[4];
    bool ok[4] = {false, false, false, false};
    for (unsigned spin = 0; spin < SPIN_CAP; ++spin) {
      MEMFENCE();
#pragma unroll
      for (int i = 0; i < 4; ++i) if (!ok[i]) v[i] = BLOAD16(a + i * 1024);
#pragma unroll
      for (int i = 0; i < 4; ++i) ok[i] = gok2(v[i], T);
      if (__all(ok[0] & ok[1] & ok[2] & ok[3])) break;
      if (POLL_SLEEP) __builtin_amdgcn_s_sleep(POLL_SLEEP);
    }
    const float s0 = parity_sum((gval(v[0].x) + gval(v[1].x)) + (gval(v[2].x) + gval(v[3].x)));
    const float s1 = parity_sum((gval(v[0].z) + gval(v[1].z)) + (gval(v[2].z) + gval(v[3].z)));
    if (lane < 2) *(GAS unsigned*)(out + wg * 4 + lane * 2) = pk2(xr0 + rbf(rbf(s0) * gate0), xr1 + rbf(rbf(s1) * gate1));
  }
  STAMP(7); STAMP_FLUSH();
}

extern "C" void kernel_launch(void* const* d_in, const int* in_sizes, int n_in, void* d_out, int out_size, void* d_ws, size_t ws_size, hipStream_t stream) {
  (void)hipFuncSetAttribute((const void*)k_fused, hipFuncAttributeMaxDynamicSharedMemorySize, LDS_BYTES);
  k_fused<<<NWG, 512, LDS_BYTES, stream>>>((const u16*)d_in[0], (const u16*)d_in[1], (const u16*)d_in[2], (const u16*)d_in[3], (const u16*)d_in[4],
                                           (const u16*)d_in[6], (const u16*)d_in[7], (const u16*)d_in[8], (u16*)d_out, (unsigned char*)d_ws);
}
```

```cpp
#define KPOL " nt"
#define VPOL ""
#include <hip/hip_runtime.h>
#include <hip/hip_bf16.h>
#include <stdint.h>

#define GAS __attribute__((address_space(1)))
#define LAS __attribute__((address_space(3)))
typedef unsigned v4u __attribute__((ext_vector_type(4)));
typedef unsigned v2u __attribute__((ext_vector_type(2)));
typedef float v4f __attribute__((ext_vector_type(4)));
typedef float v2f __attribute__((ext_vector_type(2)));
typedef __bf16 v8bf __attribute__((ext_vector_type(8)));
typedef __bf16 v2bf __attribute__((ext_vector_type(2)));
typedef short v4s __attribute__((ext_vector_type(4)));
typedef short v8s __attribute__((ext_vector_type(8)));
typedef unsigned short u16;
typedef unsigned long long u64;

constexpr int DM = 512, SK = 32768, FF = 1792;
constexpr int NWG = 256, NSPLIT = 16;
constexpr float SCALE = 0.088388347648318440f, LOG2E = 1.4426950408889634f;
constexpr int SW = 4, RSLOT = 4;
#define NSTAT 24
#define DEPTH 2
#define NO_MLP 0
#define NO_STREAM 0
#define NO_SWZ 0
#define POLL_SLEEP 0
#define WO_WHEN 2
constexpr int NT_CLASS = SK / 16 / SW, DYN0 = NSTAT * NSPLIT;

constexpr int L_RING = 0, L_STAGE = SW * RSLOT * 8192, L_XN = L_STAGE + 16384, L_QL = L_XN + 2048, L_MISC = L_QL + 1024, L_H = L_MISC + 256, L_OP = L_H + 256, L_QP = L_OP + 256,
              LDS_BYTES = L_QP + 512;
constexpr unsigned W_EPOCH = 0, W_CTR = 4096, W_QG = W_CTR + 64 * 256, W_HG = W_QG + 2 * 4096 * 8, W_IN1 = W_HG + 16 * FF * 8, W_INY = W_IN1 + NWG * 16 * 320, W_INZ = W_INY + NWG * 4096,
                   W_END = W_INZ + NWG * 2048;
constexpr unsigned SPIN_CAP = 1u << 16;

__device__ __forceinline__ float bf2f(unsigned v) { return __uint_as_float(v << 16); }
__device__ __forceinline__ u16 f2bf(float f) { __bf16 b = (__bf16)f; return __builtin_bit_cast(u16, b); }
__device__ __forceinline__ float rbf(float f) { return bf2f(f2bf(f)); }
__device__ __forceinline__ unsigned pk2(float lo, float hi) { v2bf p = {(__bf16)lo, (__bf16)hi}; return __builtin_bit_cast(unsigned, p); }
__device__ __forceinline__ float lo16(unsigned v) { return __uint_as_float(v << 16); }
__device__ __forceinline__ float hi16(unsigned v) { return __uint_as_float(v & 0xffff0000u); }
template <int CTRL> __device__ __forceinline__ float dpp(float x) {
  return __builtin_bit_cast(float, __builtin_amdgcn_mov_dpp(__builtin_bit_cast(int, x), CTRL, 0xf, 0xf, true));
}
constexpr int XOR1 = 0xB1, XOR2 = 0x4E, HMIR = 0x141, MIR = 0x140, ROR2 = 0x122, ROR4 = 0x124, ROR8 = 0x128;
__device__ __forceinline__ float swap32_add(float a, float b) { auto s = __builtin_amdgcn_permlane32_swap(__float_as_uint(a), __float_as_uint(b), false, false); return __uint_as_float(s[0]) + __uint_as_float(s[1]); }
__device__ __forceinline__ float swap16_add(float a, float b) { auto s = __builtin_amdgcn_permlane16_swap(__float_as_uint(a), __float_as_uint(b), false, false); return __uint_as_float(s[0]) + __uint_as_float(s[1]); }
__device__ __forceinline__ float xrow16_max(float x) {
  auto s = __builtin_amdgcn_permlane16_swap(__float_as_uint(x), __float_as_uint(x), false, false);
  x = fmaxf(__uint_as_float(s[0]), __uint_as_float(s[1]));
  auto t = __builtin_amdgcn_permlane32_swap(__float_as_uint(x), __float_as_uint(x), false, false);
  return fmaxf(__uint_as_float(t[0]), __uint_as_float(t[1]));
}
__device__ __forceinline__ float xrow16_sum(float x) { x = swap16_add(x, x); return swap32_add(x, x); }
__device__ __forceinline__ float row_sum(float x) { x += dpp<XOR1>(x); x += dpp<XOR2>(x); x += dpp<HMIR>(x); x += dpp<MIR>(x); return x; }
__device__ __forceinline__ float row_max(float x) { x = fmaxf(x, dpp<XOR1>(x)); x = fmaxf(x, dpp<XOR2>(x)); x = fmaxf(x, dpp<HMIR>(x)); x = fmaxf(x, dpp<MIR>(x)); return x; }
__device__ __forceinline__ float wave_sum(float x) { return xrow16_sum(row_sum(x)); }
__device__ __forceinline__ float parity_sum(float x) { x += dpp<ROR2>(x); x += dpp<ROR4>(x); x += dpp<ROR8>(x); return xrow16_sum(x); }
__device__ __forceinline__ float silu_f(float x) { return x * __builtin_amdgcn_rcpf(1.f + __builtin_amdgcn_exp2f(-x * LOG2E)); }
__device__ __forceinline__ float dot2(unsigned a, unsigned b, float c) { return __builtin_amdgcn_fdot2_f32_bf16(__builtin_bit_cast(v2bf, a), __builtin_bit_cast(v2bf, b), c, false); }

#define STAMP(i) do {} while (0)
#define STAMP_DECL do {} while (0)
#define STAMP_FLUSH() do {} while (0)

struct Tag { unsigned lo, hi; };
__device__ __forceinline__ v4u gran2(float a, float b, Tag t) { return v4u{(__float_as_uint(a) >> 8) | t.lo, t.hi, (__float_as_uint(b) >> 8) | t.lo, t.hi}; }
__device__ __forceinline__ bool gok2(v4u v, Tag t) { return (v.y == t.hi) & (v.w == t.hi) & (((v.x ^ t.lo) | (v.z ^ t.lo)) < 0x1000000u); }
__device__ __forceinline__ bool gok1(v2u v, Tag t) { return (v.y == t.hi) & ((v.x ^ t.lo) < 0x1000000u); }
__device__ __forceinline__ float gval(unsigned lo) { return __uint_as_float(lo << 8); }
#define BLOAD16(off) __builtin_amdgcn_raw_buffer_load_b128(rs, (off), 0, 16)
#define BLOAD8(off) __builtin_amdgcn_raw_buffer_load_b64(rs, (off), 0, 16)
#define BSTORE16(v, off) __builtin_amdgcn_raw_buffer_store_b128((v), rs, (off), 0, 16)
#define MEMFENCE() asm volatile("" ::: "memory")
#define LGKM0() asm volatile("s_waitcnt lgkmcnt(0)" ::: "memory")
#define BARRIER() do { LGKM0(); __builtin_amdgcn_s_barrier(); MEMFENCE(); } while (0)
#define LDSP(T, a) ((LAS T*)(uintptr_t)(a))

#define DMA_TILE(kb, vb, ldsaddr)                                                              \
  asm volatile("s_mov_b32 %0, m0\n\t"                                                           \
               "s_mov_b32 m0, %7\n\t"                                                           \
               "s_nop 0\n\t"                                                                    \
               "global_load_lds_dwordx4 %1, %5" KPOL "\n\t"                                        \
               "global_load_lds_dwordx4 %2, %5 offset:1024" KPOL "\n\t"                              \
               "global_load_lds_dwordx4 %1, %5 offset:2048" KPOL "\n\t"                              \
               "global_load_lds_dwordx4 %2, %5 offset:3072" KPOL "\n\t"                              \
               "s_add_u32 m0, m0, 0x1000\n\t"                                                   \
               "s_nop 0\n\t"                                                                    \
               "global_load_lds_dwordx4 %3, %6" VPOL "\n\t"                                        \
               "global_load_lds_dwordx4 %4, %6 offset:1024" VPOL "\n\t"                              \
               "global_load_lds_dwordx4 %3, %6 offset:2048" VPOL "\n\t"                              \
               "global_load_lds_dwordx4 %4, %6 offset:3072" VPOL "\n\t"                              \
               "s_mov_b32 m0, %0"                                                               \
               : "=&s"(m0keep) : "v"(koe), "v"(koo), "v"(voe), "v"(voo), "s"(kb), "s"(vb), "s"(ldsaddr) : "memory", "scc")

__device__ __forceinline__ void dma1(const void* gsrc, unsigned lds_dst) {
  unsigned keep;
  asm volatile("s_mov_b32 %0, m0\n\ts_mov_b32 m0, %2\n\ts_nop 0\n\tglobal_load_lds_dwordx4 %1, off nt\n\ts_mov_b32 m0, %0" : "=&s"(keep) : "v"(gsrc), "s"(lds_dst) : "memory");
}
__device__ __forceinline__ v4f mfma_tr(unsigned a_lo, unsigned a_hi, v8s bb, v4f acc) {
  const v4s a0 = __builtin_amdgcn_ds_read_tr16_b64_v4i16(LDSP(v4s, a_lo));
  const v4s a1 = __builtin_amdgcn_ds_read_tr16_b64_v4i16(LDSP(v4s, a_hi));
  const v8s a = __builtin_shufflevector(a0, a1, 0, 1, 2, 3, 4, 5, 6, 7);
  return __builtin_amdgcn_mfma_f32_16x16x32_bf16(__builtin_bit_cast(v8bf, a), __builtin_bit_cast(v8bf, bb), acc, 0, 0, 0);
}

__global__ void __launch_bounds__(512, 2) k_fused(const u16* __restrict__ x, const u16* __restrict__ Kc, const u16* __restrict__ Vc, const u16* __restrict__ wq,
                                                  const u16* __restrict__ wo, const u16* __restrict__ w1, const u16* __restrict__ w3, const u16* __restrict__ w2,
                                                  u16* __restrict__ out, unsigned char* __restrict__ ws) {
  extern __shared__ __attribute__((aligned(16))) unsigned char lds[];
  const int tid = threadIdx.x, lane = tid & 63, wave = __builtin_amdgcn_readfirstlane(tid >> 6);
  const int wg = blockIdx.x, grp = wg >> 4, sp = wg & 15, b = grp >> 3, kvh = grp & 7;
  const unsigned ldsbase = (unsigned)(uintptr_t)(LAS unsigned char*)lds;
  const __amdgpu_buffer_rsrc_t rs = __builtin_amdgcn_make_buffer_rsrc(ws, 0, W_END, 0x00020000);
  const int n = lane & 15, g = lane >> 4;
  STAMP_DECL; STAMP(0);

  if (wave < SW) {
    const unsigned ring = ldsbase + L_RING + wave * (RSLOT * 8192);
    const unsigned r4 = lane >> 4, pos = lane & 15;
    const unsigned koe = NO_SWZ ? lane * 16 : r4 * 256 + ((pos & 8) | ((pos & 7) ^ r4)) * 16;
    const unsigned koo = NO_SWZ ? lane * 16 : r4 * 256 + ((pos & 8) | ((pos & 7) ^ (4 + r4))) * 16;
    const unsigned voe = NO_SWZ ? lane * 16 : r4 * 256 + (pos ^ (r4 << 1)) * 16;
    const unsigned voo = NO_SWZ ? lane * 16 : r4 * 256 + (pos ^ ((4 + r4) << 1)) * 16;
    const size_t gbase = (size_t)grp * SK * 256 + (size_t)wave * 4096;
    const int t0s = sp * NSTAT;
    const GAS unsigned* ctr = (const GAS unsigned*)(ws + W_CTR + (grp * SW + wave) * 256);
    const unsigned one = 1;
    const char* kptr = (const char*)Kc + gbase;
    const char* vptr = (const char*)Vc + gbase;
    unsigned m0keep;
#define ISSUE(t, slot) do { if (NO_STREAM) break; const char* k0_ = kptr + (size_t)(t) * (SW * 4096); const char* v0_ = vptr + (size_t)(t) * (SW * 4096); const unsigned l_ = ring + (slot) * 8192; DMA_TILE(k0_, v0_, l_); } while (0)
#define TICKET(par) do { if (par) asm volatile("s_mov_b64 exec, 1\n\tglobal_atomic_add v251, %0, %1, off sc0\n\ts_mov_b64 exec, -1" :: "v"(ctr), "v"(one) : "memory", "v251"); \
                        else asm volatile("s_mov_b64 exec, 1\n\tglobal_atomic_add v250, %0, %1, off sc0\n\ts_mov_b64 exec, -1" :: "v"(ctr), "v"(one) : "memory", "v250"); } while (0)
    __builtin_amdgcn_s_barrier();
    ISSUE(t0s, 0); ISSUE(t0s + 1, 1);
    if (DEPTH == 3) { ISSUE(t0s + 2, 2); asm volatile("s_waitcnt vmcnt(16)" ::: "memory"); STAMP(1); ISSUE(t0s + 3, 3); }
    else {
      asm volatile("s_waitcnt vmcnt(8)" ::: "memory");
      STAMP(1);
      ISSUE(t0s + 2, 2);
      asm volatile("s_waitcnt vmcnt(8)" ::: "memory");
      ISSUE(t0s + 3, 3);
    }
    unsigned ka[4], va[8];
#pragma unroll
    for (int t = 0; t < 4; ++t) ka[t] = ring + n * 256 + ((8 * (g & 1)) | ((4 * (g >> 1) + t) ^ (n & 7))) * 16;
    { const unsigned q4 = (lane >> 2) & 3, p = lane & 3, row = 4 * g + q4;
#pragma unroll
      for (int dt = 0; dt < 8; ++dt) va[dt] = ring + 4096 + row * 256 + ((dt ^ (row & 7)) << 5) + p * 8; }
    STAMP(2);
    BARRIER();
    STAMP(3);
    v8bf qf[4];
#pragma unroll
    for (int t = 0; t < 4; ++t) qf[t] = __builtin_bit_cast(v8bf, *LDSP(const v4u, ldsbase + L_QL + (n & 3) * 256 + (8 * (g & 1) + 4 * (g >> 1) + t) * 16));
    const v2u tg = *LDSP(const v2u, ldsbase + L_MISC);
    const Tag T{tg.x, tg.y};
    float m = -INFINITY, l = 0.f;
    v4f O[8];
#pragma unroll
    for (int dt = 0; dt < 8; ++dt) O[dt] = v4f{0, 0, 0, 0};
    int i = 0, rem = NO_STREAM ? 0 : 3; bool live = !NO_STREAM;
    for (;;) {
      bool done = false;
#pragma unroll
      for (int u = 0; u < 4; ++u) {
        if (done) break;
        if (i >= RSLOT - DEPTH) {
          if (rem >= 2 && DEPTH == 3) asm volatile("s_waitcnt vmcnt(16)" ::: "memory");
          else if (rem >= 1) asm volatile("s_waitcnt vmcnt(8)" ::: "memory");
          else asm volatile("s_waitcnt vmcnt(0)" ::: "memory");
          if (live) {
            int t = t0s + i + DEPTH;
            if (i + DEPTH >= NSTAT) {
              int tk = NT_CLASS;
              if (DYN0 < NT_CLASS) { if ((u - (DEPTH - 1)) & 1) asm volatile("v_readfirstlane_b32 %0, v251" : "=s"(tk) :: "memory"); else asm volatile("v_readfirstlane_b32 %0, v250" : "=s"(tk) :: "memory"); }
              t = DYN0 + tk;
              live = (unsigned)tk < (unsigned)(NT_CLASS - DYN0);
            }
            if (live) {
              if (i + 2 * DEPTH - 1 >= NSTAT && DYN0 < NT_CLASS) TICKET(u & 1);
              ISSUE(t, (u + DEPTH) & 3);
              ++rem;
            }
          }
        }
        const unsigned so = u * 8192;
        v4u kf[4]; v4s vf[8];
#pragma unroll
        for (int t = 0; t < 4; ++t) kf[t] = *LDSP(const v4u, ka[t] + so);
#pragma unroll
        for (int dt = 0; dt < 8; ++dt) vf[dt] = __builtin_amdgcn_ds_read_tr16_b64_v4i16(LDSP(v4s, va[dt] + so));
        v4f S = {0, 0, 0, 0};
#pragma unroll
        for (int t = 0; t < 4; ++t) S = __builtin_amdgcn_mfma_f32_16x16x32_bf16(__builtin_bit_cast(v8bf, kf[t]), qf[t], S, 0, 0, 0);
        const unsigned a01 = pk2(S[0], S[1]), a23 = pk2(S[2], S[3]);
        const unsigned c01 = pk2(lo16(a01) * SCALE, hi16(a01) * SCALE), c23 = pk2(lo16(a23) * SCALE, hi16(a23) * SCALE);
        const float s0 = lo16(c01), s1 = hi16(c01), s2 = lo16(c23), s3 = hi16(c23);
        const float tm = xrow16_max(fmaxf(fmaxf(s0, s1), fmaxf(s2, s3)));
        if (__any(tm > m)) {
          const float mn = fmaxf(m, tm);
          const float alpha = __builtin_amdgcn_exp2f((m - mn) * LOG2E);
          l *= alpha;
#pragma unroll
          for (int dt = 0; dt < 8; ++dt) O[dt] *= alpha;
          m = mn;
        }
        const float p0 = __builtin_amdgcn_exp2f((s0 - m) * LOG2E), p1 = __builtin_amdgcn_exp2f((s1 - m) * LOG2E);
        const float p2 = __builtin_amdgcn_exp2f((s2 - m) * LOG2E), p3 = __builtin_amdgcn_exp2f((s3 - m) * LOG2E);
        l += (p0 + p1) + (p2 + p3);
        const v2u pp = {pk2(p0, p1), pk2(p2, p3)};
        const v4s pf = __builtin_bit_cast(v4s, pp);
#pragma unroll
        for (int dt = 0; dt < 8; ++dt) O[dt] = __builtin_amdgcn_mfma_f32_16x16x16bf16_1k(vf[dt], pf, O[dt], 0, 0, 0);
        LGKM0();
        if (rem == 0) done = true;
        --rem; ++i;
      }
      if (done) break;
    }
    STAMP(4);
    l = xrow16_sum(l);
    if (n < 4) {
      const unsigned r = ring + n * 528;
#pragma unroll
      for (int dt = 0; dt < 8; ++dt) *LDSP(v4f, r + 16 + (16 * dt + 4 * g) * 4) = O[dt];
      if (g == 0) *LDSP(v2f, r) = v2f{m, l};
    }
    BARRIER();
    STAMP(5);
    {
      const int h = wave;
      v2f ml[4], ov[4];
#pragma unroll
      for (int w = 0; w < 4; ++w) { const unsigned r = ldsbase + L_RING + w * (RSLOT * 8192) + h * 528; ml[w] = *LDSP(const v2f, r); ov[w] = *LDSP(const v2f, r + 16 + lane * 8); }
      const float M = fmaxf(fmaxf(ml[0].x, ml[1].x), fmaxf(ml[2].x, ml[3].x));
      float L = 0, o0 = 0, o1 = 0;
#pragma unroll
      for (int w = 0; w < 4; ++w) { const float wt = __builtin_amdgcn_exp2f((ml[w].x - M) * LOG2E); L = fmaf(wt, ml[w].y, L); o0 = fmaf(wt, ov[w].x, o0); o1 = fmaf(wt, ov[w].y, o1); }
      const unsigned rec = W_IN1 + ((grp * 16 + 4 * h + g) * 16 + sp) * 320;
      BSTORE16(gran2(o0, o1, T), rec + n * 16);
      if (n == 0) BSTORE16(gran2(M, L, T), rec + 256);
    }
    STAMP(6); STAMP_FLUSH();
    return;
  }

  const int hw = wave - SW;
  Tag T;
  unsigned wp[4][8];
#define LOAD_WO() do { const int row0 = (kvh * 4 + (sp >> 2)) * 128 + (sp & 3) * 32 + 8 * g; v4u w_[8];                                                              \
    _Pragma("unroll") for (int i = 0; i < 8; ++i) w_[i] = __builtin_nontemporal_load((const GAS v4u*)(wo + (size_t)(row0 + i) * DM + 128 * hw + 8 * n));              \
    _Pragma("unroll") for (int k = 0; k < 4; ++k) _Pragma("unroll") for (int j = 0; j < 4; ++j) {                                                                   \
      wp[k][2 * j] = __builtin_amdgcn_perm(w_[2 * k + 1][j], w_[2 * k][j], 0x05040100u); wp[k][2 * j + 1] = __builtin_amdgcn_perm(w_[2 * k + 1][j], w_[2 * k][j], 0x07060302u); } \
    _Pragma("unroll") for (int k = 0; k < 4; ++k) _Pragma("unroll") for (int j = 0; j < 8; ++j) asm volatile("" : "+v"(wp[k][j])); } while (0)
  {
    const int sig = (wg & 7) * 32 + (wg >> 3);
    v4u x0, x1; v2u ep;
    { const u16* xp = x + lane * 8; const unsigned char* e = ws + W_EPOCH;
      asm volatile("global_load_dwordx4 %0, %3, off nt\n\tglobal_load_dwordx4 %1, %3, off offset:1024 nt\n\tglobal_load_dwordx2 %2, %4, off nt" : "=&v"(x0), "=&v"(x1), "=&v"(ep) : "v"(xp), "v"(e) : "memory"); }
    { const char* src = (const char*)wq + (size_t)(128 * hw + (lane >> 1)) * 8192 + sig * 32 + (lane & 1) * 16;
#pragma unroll
      for (int i = 0; i < 4; ++i) dma1(src + (size_t)i * (32 * 8192), ldsbase + L_STAGE + (4 * hw + i) * 1024); }
    if (hw == 0 && lane == 0) { *LDSP(v4u, ldsbase + L_QP) = v4u{0, 0, 0, 0}; *LDSP(unsigned, ldsbase + L_OP + 64) = 0u; }
    BARRIER();
    asm volatile("s_waitcnt vmcnt(4)" : "+v"(x0), "+v"(x1), "+v"(ep) :: "memory");
    STAMP(1);
    if (hw == 0 && wg < 16 && lane < SW)
      __builtin_amdgcn_raw_buffer_store_b32(0u, rs, W_CTR + (((wg >> 3) * 8 + (wg & 7)) * SW + lane) * 256, 0, 16);
    const u64 tv = (((u64)ep.y << 32) | ep.x) + 1;
    T = Tag{(unsigned)(tv & 0xff) << 24, (unsigned)(tv >> 8)};
    {
      float v[2][8];
#pragma unroll
      for (int j = 0; j < 4; ++j) { v[0][2 * j] = lo16(x0[j]); v[0][2 * j + 1] = hi16(x0[j]); v[1][2 * j] = lo16(x1[j]); v[1][2 * j + 1] = hi16(x1[j]); }
#pragma unroll
      for (int bb = 0; bb < 2; ++bb) {
        const float s = ((v[bb][0] + v[bb][1]) + (v[bb][2] + v[bb][3])) + ((v[bb][4] + v[bb][5]) + (v[bb][6] + v[bb][7]));
        const float mean = wave_sum(s) * (1.f / DM);
        float q = 0;
#pragma unroll
        for (int j = 0; j < 8; ++j) { const float d = v[bb][j] - mean; q = fmaf(d, d, q); }
        const float var = wave_sum(q) * (1.f / DM);
        const float mb = rbf(mean);
        const float den = rbf(__builtin_amdgcn_sqrtf(rbf(rbf(var) + 1e-5f)));
        const float inv = __builtin_amdgcn_rcpf(den);
        v4u o;
#pragma unroll
        for (int j = 0; j < 4; ++j) o[j] = pk2(rbf(v[bb][2 * j] - mb) * inv, rbf(v[bb][2 * j + 1] - mb) * inv);
        const unsigned xa = ldsbase + L_XN + bb * 1024 + (lane >> 2) * 64 + (lane & 1) * 32 + ((lane >> 1) & 1) * 8;
        if (g == hw) { *LDSP(v2u, xa) = v2u{o[0], o[1]}; *LDSP(v2u, xa + 16) = v2u{o[2], o[3]}; }
      }
    }
    asm volatile("s_waitcnt vmcnt(0) lgkmcnt(0)" ::: "memory");
    STAMP(2);
    v4f q;
    {
      const unsigned aaddr = ldsbase + L_STAGE + hw * 4096 + lane * 8, baddr = ldsbase + L_XN + (lane & 1) * 1024 + hw * 256 + g * 16;
      v4f a0 = {0, 0, 0, 0}, a1 = {0, 0, 0, 0};
      a0 = mfma_tr(aaddr, aaddr + 512, *LDSP(const v8s, baddr), a0);
      a1 = mfma_tr(aaddr + 1024, aaddr + 1536, *LDSP(const v8s, baddr + 64), a1);
      a0 = mfma_tr(aaddr + 2048, aaddr + 2560, *LDSP(const v8s, baddr + 128), a0);
      a1 = mfma_tr(aaddr + 3072, aaddr + 3584, *LDSP(const v8s, baddr + 192), a1);
      q = a0 + a1;
    }
    if (hw != 0) {
      if (n < 2) *LDSP(v4f, ldsbase + L_QP + hw * 128 + (g * 2 + n) * 16) = q;
      LGKM0();
      if (lane == 0) *LDSP(volatile unsigned, ldsbase + L_QP + hw * 4) = 1u;
      if (WO_WHEN == 0) LOAD_WO();
    } else {
      for (unsigned spin = 0; spin < (SPIN_CAP << 6); ++spin) {
        const v4u f = *LDSP(volatile v4u, ldsbase + L_QP);
        if (f.y & f.z & f.w) break;
      }
      MEMFENCE();
      const unsigned qa = ldsbase + L_QP + (g * 2 + (n & 1)) * 16;
      q = (q + *LDSP(const v4f, qa + 128)) + (*LDSP(const v4f, qa + 256) + *LDSP(const v4f, qa + 384));
      if (n < 2) {
        const unsigned a = W_QG + (n * 4096 + 16 * sig + 4 * g) * 8;
        BSTORE16(gran2(rbf(q[0]), rbf(q[1]), T), a);
        BSTORE16(gran2(rbf(q[2]), rbf(q[3]), T), a + 16);
      }
      STAMP(3);
      const unsigned a = W_QG + (b * 4096 + kvh * 512) * 8 + lane * 16;
      v4u v[4];
      bool ok[4] = {false, false, false, false};
      for (unsigned spin = 0; spin < SPIN_CAP; ++spin) {
        MEMFENCE();
#pragma unroll
        for (int i = 0; i < 4; ++i) if (!ok[i]) v[i] = BLOAD16(a + i * 1024);
#pragma unroll
        for (int i = 0; i < 4; ++i) ok[i] = gok2(v[i], T);
        if (__all(ok[0] & ok[1] & ok[2] & ok[3])) break;
        __builtin_amdgcn_s_sleep(1);
      }
#pragma unroll
      for (int i = 0; i < 4; ++i) *LDSP(unsigned, ldsbase + L_QL + i * 256 + lane * 4) = ((v[i].x >> 8) & 0xffffu) | ((v[i].z << 8) & 0xffff0000u);
      if (lane == 0) *LDSP(v2u, ldsbase + L_MISC) = v2u{T.lo, T.hi};
    }
  }
  BARRIER();
  STAMP(4);
  if (WO_WHEN == 1 || (WO_WHEN == 0 && hw == 0)) LOAD_WO();
  float gate0 = 0.f, gate1 = 0.f, xr0 = 0.f, xr1 = 0.f;
  if (NO_MLP) {
  } else if (hw == 1) {
    if (wg < 224) {
      const int mat = wg >= 112, rem = wg - 112 * mat, cbk = rem >> 2, rbk = rem & 3;
      const char* src = (const char*)(mat ? w3 : w1) + (size_t)(128 * rbk + (lane >> 3)) * (FF * 2) + cbk * 128 + (lane & 7) * 16;
#pragma unroll
      for (int i = 0; i < 16; ++i) dma1(src + (size_t)i * (8 * FF * 2), ldsbase + L_STAGE + i * 1024);
      asm volatile("s_waitcnt vmcnt(0)" ::: "memory");
      const unsigned aaddr = ldsbase + L_STAGE + (lane >> 2) * 128 + (lane & 3) * 8, baddr = ldsbase + L_XN + (lane & 1) * 1024 + rbk * 256 + g * 16;
      v4f acc[4];
#pragma unroll
      for (int ct = 0; ct < 4; ++ct) acc[ct] = v4f{0, 0, 0, 0};
#pragma unroll
      for (int s = 0; s < 4; ++s) {
        const v8s bb = *LDSP(const v8s, baddr + s * 64);
#pragma unroll
        for (int ct = 0; ct < 4; ++ct) acc[ct] = mfma_tr(aaddr + s * 4096 + ct * 32, aaddr + s * 4096 + 2048 + ct * 32, bb, acc[ct]);
      }
      if (n < 2) {
        const unsigned a = W_HG + (((mat * 2 + n) * 4 + rbk) * FF + 64 * cbk + 4 * g) * 8;
#pragma unroll
        for (int ct = 0; ct < 4; ++ct) { BSTORE16(gran2(acc[ct][0], acc[ct][1], T), a + ct * 128); BSTORE16(gran2(acc[ct][2], acc[ct][3], T), a + ct * 128 + 16); }
      }
    }
  } else if (hw == 2) {
    const int rb = wg >> 2, cb = wg & 3;
    v4u w[7];
#pragma unroll
    for (int r = 0; r < 7; ++r) w[r] = __builtin_nontemporal_load((const GAS v4u*)(w2 + (size_t)(rb * 28 + g * 7 + r) * DM + cb * 128 + n * 8));
    const int t = lane < 56 ? lane : 0, tb = t / 28, tr = t % 28;
    const unsigned a = W_HG + (tb * 4 * FF + rb * 28 + tr) * 8;
    v2u pv[8];
    __builtin_amdgcn_s_sleep(127);
    for (unsigned spin = 0; spin < SPIN_CAP; ++spin) {
      __builtin_amdgcn_s_sleep(64);
      MEMFENCE();
      bool ok = true;
#pragma unroll
      for (int i = 0; i < 8; ++i) pv[i] = BLOAD8(a + ((i >> 2) * 8 + (i & 3)) * FF * 8);
#pragma unroll
      for (int i = 0; i < 8; ++i) ok &= gok1(pv[i], T);
      if (__all(ok)) break;
    }
    const float gt = rbf((gval(pv[0].x) + gval(pv[1].x)) + (gval(pv[2].x) + gval(pv[3].x))), up = rbf((gval(pv[4].x) + gval(pv[5].x)) + (gval(pv[6].x) + gval(pv[7].x)));
    const float hval = rbf(rbf(silu_f(gt)) * up);
    if (lane < 56) *LDSP(float, ldsbase + L_H + (tb * 32 + (tr / 7) * 8 + tr % 7) * 4) = hval;
    LGKM0();
#pragma unroll
    for (int bb = 0; bb < 2; ++bb) {
      const v4f h0 = *LDSP(const v4f, ldsbase + L_H + (bb * 32 + g * 8) * 4), h1 = *LDSP(const v4f, ldsbase + L_H + (bb * 32 + g * 8 + 4) * 4);
      const float hh[7] = {h0[0], h0[1], h0[2], h0[3], h1[0], h1[1], h1[2]};
      float z[8];
#pragma unroll
      for (int c = 0; c < 8; ++c) z[c] = 0.f;
#pragma unroll
      for (int r = 0; r < 7; ++r)
#pragma unroll
        for (int j = 0; j < 4; ++j) { z[2 * j] = fmaf(hh[r], lo16(w[r][j]), z[2 * j]); z[2 * j + 1] = fmaf(hh[r], hi16(w[r][j]), z[2 * j + 1]); }
      float t4[4];
#pragma unroll
      for (int j = 0; j < 4; ++j) t4[j] = swap32_add(z[j], z[j + 4]);
      BSTORE16(gran2(swap16_add(t4[0], t4[2]), swap16_add(t4[1], t4[3]), T), W_INZ + ((bb * 128 + 32 * cb + 2 * n + (g >> 1)) * 64 + rb) * 32 + (g & 1) * 16);
    }
  } else if (hw == 3) {
    if (wg == 0) {
      for (unsigned spin = 0; spin < SPIN_CAP; ++spin) {
        __builtin_amdgcn_s_sleep(32);
        MEMFENCE();
        bool ok = true;
#pragma unroll
        for (int i = 0; i < 4; ++i) ok &= gok1(BLOAD8(W_QG + (i * 64 + lane) * 128), T);
        if (__all(ok)) break;
      }
      if (lane == 0) {
        const v2u e = BLOAD8(W_EPOCH);
        const u64 tv = (((u64)e.y << 32) | e.x) + 1;
        __builtin_amdgcn_raw_buffer_store_b64(v2u{(unsigned)tv, (unsigned)(tv >> 32)}, rs, W_EPOCH, 0, 16);
      }
    }
    const unsigned xv = __builtin_nontemporal_load((const GAS unsigned*)(x + wg * 4 + (lane & 1) * 2));
    xr0 = lo16(xv); xr1 = hi16(xv);
    const unsigned a = W_INZ + wg * 2048 + lane * 16;
    v4u v[2];
    __builtin_amdgcn_s_sleep(127); __builtin_amdgcn_s_sleep(127); __builtin_amdgcn_s_sleep(127);
    for (unsigned spin = 0; spin < SPIN_CAP; ++spin) {
      __builtin_amdgcn_s_sleep(64);
      MEMFENCE();
      v[0] = BLOAD16(a); v[1] = BLOAD16(a + 1024);
      if (__all(gok2(v[0], T) & gok2(v[1], T))) break;
    }
    const float z0 = parity_sum(gval(v[0].x) + gval(v[1].x)), z1 = parity_sum(gval(v[0].z) + gval(v[1].z));
    gate0 = rbf(silu_f(rbf(z0))); gate1 = rbf(silu_f(rbf(z1)));
  }
  if (hw != 0) STAMP(1);
  if (WO_WHEN == 2) LOAD_WO();
  if (hw != 0) STAMP(2);
  BARRIER();
  STAMP(5);
  {
    unsigned op[4];
    if (hw == 0) {
      const unsigned a = W_IN1 + (wg * 16 + n) * 320;
      v4u v[4], ml;
      bool ok = false;
      for (unsigned spin = 0; spin < SPIN_CAP; ++spin) {
        MEMFENCE();
        if (!ok) {
#pragma unroll
          for (int i = 0; i < 4; ++i) v[i] = BLOAD16(a + g * 64 + i * 16);
          ml = BLOAD16(a + 256);
          ok = gok2(v[0], T) & gok2(v[1], T) & gok2(v[2], T) & gok2(v[3], T) & gok2(ml, T);
        }
        if (__all(ok)) break;
        if (POLL_SLEEP) __builtin_amdgcn_s_sleep(POLL_SLEEP);
      }
      STAMP(6);
      const float mp = gval(ml.x), lp = gval(ml.z);
      const float M = row_max(mp);
      const float wt = __builtin_amdgcn_exp2f((mp - M) * LOG2E);
      const float invL = __builtin_amdgcn_rcpf(row_sum(wt * lp));
#pragma unroll
      for (int i = 0; i < 4; ++i) op[i] = pk2(row_sum(wt * gval(v[i].x)) * invL, row_sum(wt * gval(v[i].z)) * invL);
      if (n == 0) *LDSP(v4u, ldsbase + L_OP + g * 16) = v4u{op[0], op[1], op[2], op[3]};
      LGKM0();
      if (lane == 0) *LDSP(volatile unsigned, ldsbase + L_OP + 64) = 1u;
    } else {
      for (unsigned spin = 0; spin < (SPIN_CAP << 8); ++spin) {
        if (*LDSP(volatile unsigned, ldsbase + L_OP + 64)) break;
        __builtin_amdgcn_s_sleep(1);
      }
      MEMFENCE();
      const v4u o4 = *LDSP(const v4u, ldsbase + L_OP + g * 16);
      op[0] = o4.x; op[1] = o4.y; op[2] = o4.z; op[3] = o4.w;
      STAMP(6);
    }
    float y[8];
#pragma unroll
    for (int c = 0; c < 8; ++c) { float acc = 0.f;
#pragma unroll
      for (int k = 0; k < 4; ++k) acc = dot2(wp[k][c], op[k], acc);
      y[c] = acc; }
    float t4[4];
#pragma unroll
    for (int j = 0; j < 4; ++j) t4[j] = swap32_add(y[j], y[j + 4]);
    const unsigned a2 = W_INY + ((b * 128 + 32 * hw + 2 * n + (g >> 1)) * 128 + (wg & 127)) * 32 + (g & 1) * 16;
    BSTORE16(gran2(swap16_add(t4[0], t4[2]), swap16_add(t4[1], t4[3]), T), a2);
  }
  if (hw == 3) {
    const unsigned a = W_INY + wg * 4096 + lane * 16;
    v4u v[4];
    bool ok[4] = {false, false, false, false};
    for (unsigned spin = 0; spin < SPIN_CAP; ++spin) {
      MEMFENCE();
#pragma unroll
      for (int i = 0; i < 4; ++i) if (!ok[i]) v[i] = BLOAD16(a + i * 1024);
#pragma unroll
      for (int i = 0; i < 4; ++i) ok[i] = gok2(v[i], T);
      if (__all(ok[0] & ok[1] & ok[2] & ok[3])) break;
      if (POLL_SLEEP) __builtin_amdgcn_s_sleep(POLL_SLEEP);
    }
    const float s0 = parity_sum((gval(v[0].x) + gval(v[1].x)) + (gval(v[2].x) + gval(v[3].x)));
    const float s1 = parity_sum((gval(v[0].z) + gval(v[1].z)) + (gval(v[2].z) + gval(v[3].z)));
    if (lane < 2) *(GAS unsigned*)(out + wg * 4 + lane * 2) = pk2(xr0 + rbf(rbf(s0) * gate0), xr1 + rbf(rbf(s1) * gate1));
  }
  STAMP(7); STAMP_FLUSH();
}

extern "C" void kernel_launch(void* const* d_in, const int* in_sizes, int n_in, void* d_out, int out_size, void* d_ws, size_t ws_size, hipStream_t stream) {
  (void)hipFuncSetAttribute((const void*)k_fused, hipFuncAttributeMaxDynamicSharedMemorySize, LDS_BYTES);
  k_fused<<<NWG, 512, LDS_BYTES, stream>>>((const u16*)d_in[0], (const u16*)d_in[1], (const u16*)d_in[2], (const u16*)d_in[3], (const u16*)d_in[4],
                                           (const u16*)d_in[6], (const u16*)d_in[7], (const u16*)d_in[8], (u16*)d_out, (unsigned char*)d_ws);
}
```

```cpp
#define POLK_EXPR (grp < 8)
#define POLV_EXPR (grp < 8)
#include <hip/hip_runtime.h>
#include <hip/hip_bf16.h>
#include <stdint.h>

#define GAS __attribute__((address_space(1)))
#define LAS __attribute__((address_space(3)))
typedef unsigned v4u __attribute__((ext_vector_type(4)));
typedef unsigned v2u __attribute__((ext_vector_type(2)));
typedef float v4f __attribute__((ext_vector_type(4)));
typedef float v2f __attribute__((ext_vector_type(2)));
typedef __bf16 v8bf __attribute__((ext_vector_type(8)));
typedef __bf16 v2bf __attribute__((ext_vector_type(2)));
typedef short v4s __attribute__((ext_vector_type(4)));
typedef short v8s __attribute__((ext_vector_type(8)));
typedef unsigned short u16;
typedef unsigned long long u64;

constexpr int DM = 512, SK = 32768, FF = 1792;
constexpr int NWG = 256, NSPLIT = 16;
constexpr float SCALE = 0.088388347648318440f, LOG2E = 1.4426950408889634f;
constexpr int SW = 4, RSLOT = 4;
#define NSTAT 24
#define KPOL " nt"
#define VPOL " nt"
#define DEPTH 2
#define NO_MLP 0
#define NO_STREAM 0
#define NO_SWZ 0
#define POLL_SLEEP 0
#define WO_WHEN 2
constexpr int NT_CLASS = SK / 16 / SW, DYN0 = NSTAT * NSPLIT;

constexpr int L_RING = 0, L_STAGE = SW * RSLOT * 8192, L_XN = L_STAGE + 16384, L_QL = L_XN + 2048, L_MISC = L_QL + 1024, L_H = L_MISC + 256, L_OP = L_H + 256, L_QP = L_OP + 256,
              LDS_BYTES = L_QP + 512;
constexpr unsigned W_EPOCH = 0, W_CTR = 4096, W_QG = W_CTR + 64 * 256, W_HG = W_QG + 2 * 4096 * 8, W_IN1 = W_HG + 16 * FF * 8, W_INY = W_IN1 + NWG * 16 * 320, W_INZ = W_INY + NWG * 4096,
                   W_END = W_INZ + NWG * 2048;
constexpr unsigned SPIN_CAP = 1u << 16;

__device__ __forceinline__ float bf2f(unsigned v) { return __uint_as_float(v << 16); }
__device__ __forceinline__ u16 f2bf(float f) { __bf16 b = (__bf16)f; return __builtin_bit_cast(u16, b); }
__device__ __forceinline__ float rbf(float f) { return bf2f(f2bf(f)); }
__device__ __forceinline__ unsigned pk2(float lo, float hi) { v2bf p = {(__bf16)lo, (__bf16)hi}; return __builtin_bit_cast(unsigned, p); }
__device__ __forceinline__ float lo16(unsigned v) { return __uint_as_float(v << 16); }
__device__ __forceinline__ float hi16(unsigned v) { return __uint_as_float(v & 0xffff0000u); }
template <int CTRL> __device__ __forceinline__ float dpp(float x) {
  return __builtin_bit_cast(float, __builtin_amdgcn_mov_dpp(__builtin_bit_cast(int, x), CTRL, 0xf, 0xf, true));
}
constexpr int XOR1 = 0xB1, XOR2 = 0x4E, HMIR = 0x141, MIR = 0x140, ROR2 = 0x122, ROR4 = 0x124, ROR8 = 0x128;
__device__ __forceinline__ float swap32_add(float a, float b) { auto s = __builtin_amdgcn_permlane32_swap(__float_as_uint(a), __float_as_uint(b), false, false); return __uint_as_float(s[0]) + __uint_as_float(s[1]); }
__device__ __forceinline__ float swap16_add(float a, float b) { auto s = __builtin_amdgcn_permlane16_swap(__float_as_uint(a), __float_as_uint(b), false, false); return __uint_as_float(s[0]) + __uint_as_float(s[1]); }
__device__ __forceinline__ float xrow16_max(float x) {
  auto s = __builtin_amdgcn_permlane16_swap(__float_as_uint(x), __float_as_uint(x), false, false);
  x = fmaxf(__uint_as_float(s[0]), __uint_as_float(s[1]));
  auto t = __builtin_amdgcn_permlane32_swap(__float_as_uint(x), __float_as_uint(x), false, false);
  return fmaxf(__uint_as_float(t[0]), __uint_as_float(t[1]));
}
__device__ __forceinline__ float xrow16_sum(float x) { x = swap16_add(x, x); return swap32_add(x, x); }
__device__ __forceinline__ float row_sum(float x) { x += dpp<XOR1>(x); x += dpp<XOR2>(x); x += dpp<HMIR>(x); x += dpp<MIR>(x); return x; }
__device__ __forceinline__ float row_max(float x) { x = fmaxf(x, dpp<XOR1>(x)); x = fmaxf(x, dpp<XOR2>(x)); x = fmaxf(x, dpp<HMIR>(x)); x = fmaxf(x, dpp<MIR>(x)); return x; }
__device__ __forceinline__ float wave_sum(float x) { return xrow16_sum(row_sum(x)); }
__device__ __forceinline__ float parity_sum(float x) { x += dpp<ROR2>(x); x += dpp<ROR4>(x); x += dpp<ROR8>(x); return xrow16_sum(x); }
__device__ __forceinline__ float silu_f(float x) { return x * __builtin_amdgcn_rcpf(1.f + __builtin_amdgcn_exp2f(-x * LOG2E)); }
__device__ __forceinline__ float dot2(unsigned a, unsigned b, float c) { return __builtin_amdgcn_fdot2_f32_bf16(__builtin_bit_cast(v2bf, a), __builtin_bit_cast(v2bf, b), c, false); }

#define STAMP(i) do {} while (0)
#define STAMP_DECL do {} while (0)
#define STAMP_FLUSH() do {} while (0)

struct Tag { unsigned lo, hi; };
__device__ __forceinline__ v4u gran2(float a, float b, Tag t) { return v4u{(__float_as_uint(a) >> 8) | t.lo, t.hi, (__float_as_uint(b) >> 8) | t.lo, t.hi}; }
__device__ __forceinline__ bool gok2(v4u v, Tag t) { return (v.y == t.hi) & (v.w == t.hi) & (((v.x ^ t.lo) | (v.z ^ t.lo)) < 0x1000000u); }
__device__ __forceinline__ bool gok1(v2u v, Tag t) { return (v.y == t.hi) & ((v.x ^ t.lo) < 0x1000000u); }
__device__ __forceinline__ float gval(unsigned lo) { return __uint_as_float(lo << 8); }
#define BLOAD16(off) __builtin_amdgcn_raw_buffer_load_b128(rs, (off), 0, 16)
#define BLOAD8(off) __builtin_amdgcn_raw_buffer_load_b64(rs, (off), 0, 16)
#define BSTORE16(v, off) __builtin_amdgcn_raw_buffer_store_b128((v), rs, (off), 0, 16)
#define MEMFENCE() asm volatile("" ::: "memory")
#define LGKM0() asm volatile("s_waitcnt lgkmcnt(0)" ::: "memory")
#define BARRIER() do { LGKM0(); __builtin_amdgcn_s_barrier(); MEMFENCE(); } while (0)
#define LDSP(T, a) ((LAS T*)(uintptr_t)(a))

#define DMA_HALF(POL, oe, oo, gb, ldsaddr)                                                       \
  asm volatile("s_mov_b32 %0, m0\n\t"                                                           \
               "s_mov_b32 m0, %4\n\t"                                                           \
               "s_nop 0\n\t"                                                                    \
               "global_load_lds_dwordx4 %1, %3" POL "\n\t"                                      \
               "global_load_lds_dwordx4 %2, %3 offset:1024" POL "\n\t"                          \
               "global_load_lds_dwordx4 %1, %3 offset:2048" POL "\n\t"                          \
               "global_load_lds_dwordx4 %2, %3 offset:3072" POL "\n\t"                          \
               "s_mov_b32 m0, %0"                                                               \
               : "=&s"(m0keep) : "v"(oe), "v"(oo), "s"(gb), "s"(ldsaddr) : "memory")
#define DMA_TILE(kb, vb, ldsaddr) do { const unsigned l2_ = (ldsaddr) + 4096;                     \
    if (polk) DMA_HALF("", koe, koo, kb, ldsaddr); else DMA_HALF(" nt", koe, koo, kb, ldsaddr);   \
    if (polv) DMA_HALF("", voe, voo, vb, l2_); else DMA_HALF(" nt", voe, voo, vb, l2_); } while (0)

__device__ __forceinline__ void dma1(const void* gsrc, unsigned lds_dst) {
  unsigned keep;
  asm volatile("s_mov_b32 %0, m0\n\ts_mov_b32 m0, %2\n\ts_nop 0\n\tglobal_load_lds_dwordx4 %1, off nt\n\ts_mov_b32 m0, %0" : "=&s"(keep) : "v"(gsrc), "s"(lds_dst) : "memory");
}
__device__ __forceinline__ v4f mfma_tr(unsigned a_lo, unsigned a_hi, v8s bb, v4f acc) {
  const v4s a0 = __builtin_amdgcn_ds_read_tr16_b64_v4i16(LDSP(v4s, a_lo));
  const v4s a1 = __builtin_amdgcn_ds_read_tr16_b64_v4i16(LDSP(v4s, a_hi));
  const v8s a = __builtin_shufflevector(a0, a1, 0, 1, 2, 3, 4, 5, 6, 7);
  return __builtin_amdgcn_mfma_f32_16x16x32_bf16(__builtin_bit_cast(v8bf, a), __builtin_bit_cast(v8bf, bb), acc, 0, 0, 0);
}

__global__ void __launch_bounds__(512, 2) k_fused(const u16* __restrict__ x, const u16* __restrict__ Kc, const u16* __restrict__ Vc, const u16* __restrict__ wq,
                                                  const u16* __restrict__ wo, const u16* __restrict__ w1, const u16* __restrict__ w3, const u16* __restrict__ w2,
                                                  u16* __restrict__ out, unsigned char* __restrict__ ws) {
  extern __shared__ __attribute__((aligned(16))) unsigned char lds[];
  const int tid = threadIdx.x, lane = tid & 63, wave = __builtin_amdgcn_readfirstlane(tid >> 6);
  const int wg = blockIdx.x, grp = wg >> 4, sp = wg & 15, b = grp >> 3, kvh = grp & 7;
  const unsigned ldsbase = (unsigned)(uintptr_t)(LAS unsigned char*)lds;
  const __amdgpu_buffer_rsrc_t rs = __builtin_amdgcn_make_buffer_rsrc(ws, 0, W_END, 0x00020000);
  const int n = lane & 15, g = lane >> 4;
  STAMP_DECL; STAMP(0);

  if (wave < SW) {
    const unsigned ring = ldsbase + L_RING + wave * (RSLOT * 8192);
    const unsigned r4 = lane >> 4, pos = lane & 15;
    const unsigned koe = NO_SWZ ? lane * 16 : r4 * 256 + ((pos & 8) | ((pos & 7) ^ r4)) * 16;
    const unsigned koo = NO_SWZ ? lane * 16 : r4 * 256 + ((pos & 8) | ((pos & 7) ^ (4 + r4))) * 16;
    const unsigned voe = NO_SWZ ? lane * 16 : r4 * 256 + (pos ^ (r4 << 1)) * 16;
    const unsigned voo = NO_SWZ ? lane * 16 : r4 * 256 + (pos ^ ((4 + r4) << 1)) * 16;
    const size_t gbase = (size_t)grp * SK * 256 + (size_t)wave * 4096;
    const int t0s = sp * NSTAT;
    const GAS unsigned* ctr = (const GAS unsigned*)(ws + W_CTR + (grp * SW + wave) * 256);
    const unsigned one = 1;
    const char* kptr = (const char*)Kc + gbase;
    const char* vptr = (const char*)Vc + gbase;
    unsigned m0keep;
#define ISSUE(t, slot) do { if (NO_STREAM) break; const int tt_ = (t); const bool polk = POLK_EXPR, polv = POLV_EXPR; const char* k0_ = kptr + (size_t)(t) * (SW * 4096); const char* v0_ = vptr + (size_t)(t) * (SW * 4096); const unsigned l_ = ring + (slot) * 8192; DMA_TILE(k0_, v0_, l_); } while (0)
#define TICKET(par) do { if (par) asm volatile("s_mov_b64 exec, 1\n\tglobal_atomic_add v251, %0, %1, off sc0\n\ts_mov_b64 exec, -1" :: "v"(ctr), "v"(one) : "memory", "v251"); \
                        else asm volatile("s_mov_b64 exec, 1\n\tglobal_atomic_add v250, %0, %1, off sc0\n\ts_mov_b64 exec, -1" :: "v"(ctr), "v"(one) : "memory", "v250"); } while (0)
    __builtin_amdgcn_s_barrier();
    ISSUE(t0s, 0); ISSUE(t0s + 1, 1);
    if (DEPTH == 3) { ISSUE(t0s + 2, 2); asm volatile("s_waitcnt vmcnt(16)" ::: "memory"); STAMP(1); ISSUE(t0s + 3, 3); }
    else {
      asm volatile("s_waitcnt vmcnt(8)" ::: "memory");
      STAMP(1);
      ISSUE(t0s + 2, 2);
      asm volatile("s_waitcnt vmcnt(8)" ::: "memory");
      ISSUE(t0s + 3, 3);
    }
    unsigned ka[4], va[8];
#pragma unroll
    for (int t = 0; t < 4; ++t) ka[t] = ring + n * 256 + ((8 * (g & 1)) | ((4 * (g >> 1) + t) ^ (n & 7))) * 16;
    { const unsigned q4 = (lane >> 2) & 3, p = lane & 3, row = 4 * g + q4;
#pragma unroll
      for (int dt = 0; dt < 8; ++dt) va[dt] = ring + 4096 + row * 256 + ((dt ^ (row & 7)) << 5) + p * 8; }
    STAMP(2);
    BARRIER();
    STAMP(3);
    v8bf qf[4];
#pragma unroll
    for (int t = 0; t < 4; ++t) qf[t] = __builtin_bit_cast(v8bf, *LDSP(const v4u, ldsbase + L_QL + (n & 3) * 256 + (8 * (g & 1) + 4 * (g >> 1) + t) * 16));
    const v2u tg = *LDSP(const v2u, ldsbase + L_MISC);
    const Tag T{tg.x, tg.y};
    float m = -INFINITY, l = 0.f;
    v4f O[8];
#pragma unroll
    for (int dt = 0; dt < 8; ++dt) O[dt] = v4f{0, 0, 0, 0};
    int i = 0, rem = NO_STREAM ? 0 : 3; bool live = !NO_STREAM;
    for (;;) {
      bool done = false;
#pragma unroll
      for (int u = 0; u < 4; ++u) {
        if (done) break;
        if (i >= RSLOT - DEPTH) {
          if (rem >= 2 && DEPTH == 3) asm volatile("s_waitcnt vmcnt(16)" ::: "memory");
          else if (rem >= 1) asm volatile("s_waitcnt vmcnt(8)" ::: "memory");
          else asm volatile("s_waitcnt vmcnt(0)" ::: "memory");
          if (live) {
            int t = t0s + i + DEPTH;
            if (i + DEPTH >= NSTAT) {
              int tk = NT_CLASS;
              if (DYN0 < NT_CLASS) { if ((u - (DEPTH - 1)) & 1) asm volatile("v_readfirstlane_b32 %0, v251" : "=s"(tk) :: "memory"); else asm volatile("v_readfirstlane_b32 %0, v250" : "=s"(tk) :: "memory"); }
              t = DYN0 + tk;
              live = (unsigned)tk < (unsigned)(NT_CLASS - DYN0);
            }
            if (live) {
              if (i + 2 * DEPTH - 1 >= NSTAT && DYN0 < NT_CLASS) TICKET(u & 1);
              ISSUE(t, (u + DEPTH) & 3);
              ++rem;
            }
          }
        }
        const unsigned so = u * 8192;
        v4u kf[4]; v4s vf[8];
#pragma unroll
        for (int t = 0; t < 4; ++t) kf[t] = *LDSP(const v4u, ka[t] + so);
#pragma unroll
        for (int dt = 0; dt < 8; ++dt) vf[dt] = __builtin_amdgcn_ds_read_tr16_b64_v4i16(LDSP(v4s, va[dt] + so));
        v4f S = {0, 0, 0, 0};
#pragma unroll
        for (int t = 0; t < 4; ++t) S = __builtin_amdgcn_mfma_f32_16x16x32_bf16(__builtin_bit_cast(v8bf, kf[t]), qf[t], S, 0, 0, 0);
        const unsigned a01 = pk2(S[0], S[1]), a23 = pk2(S[2], S[3]);
        const unsigned c01 = pk2(lo16(a01) * SCALE, hi16(a01) * SCALE), c23 = pk2(lo16(a23) * SCALE, hi16(a23) * SCALE);
        const float s0 = lo16(c01), s1 = hi16(c01), s2 = lo16(c23), s3 = hi16(c23);
        const float tm = xrow16_max(fmaxf(fmaxf(s0, s1), fmaxf(s2, s3)));
        if (__any(tm > m)) {
          const float mn = fmaxf(m, tm);
          const float alpha = __builtin_amdgcn_exp2f((m - mn) * LOG2E);
          l *= alpha;
#pragma unroll
          for (int dt = 0; dt < 8; ++dt) O[dt] *= alpha;
          m = mn;
        }
        const float p0 = __builtin_amdgcn_exp2f((s0 - m) * LOG2E), p1 = __builtin_amdgcn_exp2f((s1 - m) * LOG2E);
        const float p2 = __builtin_amdgcn_exp2f((s2 - m) * LOG2E), p3 = __builtin_amdgcn_exp2f((s3 - m) * LOG2E);
        l += (p0 + p1) + (p2 + p3);
        const v2u pp = {pk2(p0, p1), pk2(p2, p3)};
        const v4s pf = __builtin_bit_cast(v4s, pp);
#pragma unroll
        for (int dt = 0; dt < 8; ++dt) O[dt] = __builtin_amdgcn_mfma_f32_16x16x16bf16_1k(vf[dt], pf, O[dt], 0, 0, 0);
        LGKM0();
        if (rem == 0) done = true;
        --rem; ++i;
      }
      if (done) break;
    }
    STAMP(4);
    l = xrow16_sum(l);
    if (n < 4) {
      const unsigned r = ring + n * 528;
#pragma unroll
      for (int dt = 0; dt < 8; ++dt) *LDSP(v4f, r + 16 + (16 * dt + 4 * g) * 4) = O[dt];
      if (g == 0) *LDSP(v2f, r) = v2f{m, l};
    }
    BARRIER();
    STAMP(5);
    {
      const int h = wave;
      v2f ml[4], ov[4];
#pragma unroll
      for (int w = 0; w < 4; ++w) { const unsigned r = ldsbase + L_RING + w * (RSLOT * 8192) + h * 528; ml[w] = *LDSP(const v2f, r); ov[w] = *LDSP(const v2f, r + 16 + lane * 8); }
      const float M = fmaxf(fmaxf(ml[0].x, ml[1].x), fmaxf(ml[2].x, ml[3].x));
      float L = 0, o0 = 0, o1 = 0;
#pragma unroll
      for (int w = 0; w < 4; ++w) { const float wt = __builtin_amdgcn_exp2f((ml[w].x - M) * LOG2E); L = fmaf(wt, ml[w].y, L); o0 = fmaf(wt, ov[w].x, o0); o1 = fmaf(wt, ov[w].y, o1); }
      const unsigned rec = W_IN1 + ((grp * 16 + 4 * h + g) * 16 + sp) * 320;
      BSTORE16(gran2(o0, o1, T), rec + n * 16);
      if (n == 0) BSTORE16(gran2(M, L, T), rec + 256);
    }
    STAMP(6); STAMP_FLUSH();
    return;
  }

  const int hw = wave - SW;
  Tag T;
  unsigned wp[4][8];
#define LOAD_WO() do { const int row0 = (kvh * 4 + (sp >> 2)) * 128 + (sp & 3) * 32 + 8 * g; v4u w_[8];                                                              \
    _Pragma("unroll") for (int i = 0; i < 8; ++i) w_[i] = __builtin_nontemporal_load((const GAS v4u*)(wo + (size_t)(row0 + i) * DM + 128 * hw + 8 * n));              \
    _Pragma("unroll") for (int k = 0; k < 4; ++k) _Pragma("unroll") for (int j = 0; j < 4; ++j) {                                                                   \
      wp[k][2 * j] = __builtin_amdgcn_perm(w_[2 * k + 1][j], w_[2 * k][j], 0x05040100u); wp[k][2 * j + 1] = __builtin_amdgcn_perm(w_[2 * k + 1][j], w_[2 * k][j], 0x07060302u); } \
    _Pragma("unroll") for (int k = 0; k < 4; ++k) _Pragma("unroll") for (int j = 0; j < 8; ++j) asm volatile("" : "+v"(wp[k][j])); } while (0)
  {
    const int sig = (wg & 7) * 32 + (wg >> 3);
    v4u x0, x1; v2u ep;
    { const u16* xp = x + lane * 8; const unsigned char* e = ws + W_EPOCH;
      asm volatile("global_load_dwordx4 %0, %3, off nt\n\tglobal_load_dwordx4 %1, %3, off offset:1024 nt\n\tglobal_load_dwordx2 %2, %4, off nt" : "=&v"(x0), "=&v"(x1), "=&v"(ep) : "v"(xp), "v"(e) : "memory"); }
    { const char* src = (const char*)wq + (size_t)(128 * hw + (lane >> 1)) * 8192 + sig * 32 + (lane & 1) * 16;
#pragma unroll
      for (int i = 0; i < 4; ++i) dma1(src + (size_t)i * (32 * 8192), ldsbase + L_STAGE + (4 * hw + i) * 1024); }
    if (hw == 0 && lane == 0) { *LDSP(v4u, ldsbase + L_QP) = v4u{0, 0, 0, 0}; *LDSP(unsigned, ldsbase + L_OP + 64) = 0u; }
    BARRIER();
    asm volatile("s_waitcnt vmcnt(4)" : "+v"(x0), "+v"(x1), "+v"(ep) :: "memory");
    STAMP(1);
    if (hw == 0 && wg < 16 && lane < SW)
      __builtin_amdgcn_raw_buffer_store_b32(0u, rs, W_CTR + (((wg >> 3) * 8 + (wg & 7)) * SW + lane) * 256, 0, 16);
    const u64 tv = (((u64)ep.y << 32) | ep.x) + 1;
    T = Tag{(unsigned)(tv & 0xff) << 24, (unsigned)(tv >> 8)};
    {
      float v[2][8];
#pragma unroll
      for (int j = 0; j < 4; ++j) { v[0][2 * j] = lo16(x0[j]); v[0][2 * j + 1] = hi16(x0[j]); v[1][2 * j] = lo16(x1[j]); v[1][2 * j + 1] = hi16(x1[j]); }
#pragma unroll
      for (int bb = 0; bb < 2; ++bb) {
        const float s = ((v[bb][0] + v[bb][1]) + (v[bb][2] + v[bb][3])) + ((v[bb][4] + v[bb][5]) + (v[bb][6] + v[bb][7]));
        const float mean = wave_sum(s) * (1.f / DM);
        float q = 0;
#pragma unroll
        for (int j = 0; j < 8; ++j) { const float d = v[bb][j] - mean; q = fmaf(d, d, q); }
        const float var = wave_sum(q) * (1.f / DM);
        const float mb = rbf(mean);
        const float den = rbf(__builtin_amdgcn_sqrtf(rbf(rbf(var) + 1e-5f)));
        const float inv = __builtin_amdgcn_rcpf(den);
        v4u o;
#pragma unroll
        for (int j = 0; j < 4; ++j) o[j] = pk2(rbf(v[bb][2 * j] - mb) * inv, rbf(v[bb][2 * j + 1] - mb) * inv);
        const unsigned xa = ldsbase + L_XN + bb * 1024 + (lane >> 2) * 64 + (lane & 1) * 32 + ((lane >> 1) & 1) * 8;
        if (g == hw) { *LDSP(v2u, xa) = v2u{o[0], o[1]}; *LDSP(v2u, xa + 16) = v2u{o[2], o[3]}; }
      }
    }
    asm volatile("s_waitcnt vmcnt(0) lgkmcnt(0)" ::: "memory");
    STAMP(2);
    v4f q;
    {
      const unsigned aaddr = ldsbase + L_STAGE + hw * 4096 + lane * 8, baddr = ldsbase + L_XN + (lane & 1) * 1024 + hw * 256 + g * 16;
      v4f a0 = {0, 0, 0, 0}, a1 = {0, 0, 0, 0};
      a0 = mfma_tr(aaddr, aaddr + 512, *LDSP(const v8s, baddr), a0);
      a1 = mfma_tr(aaddr + 1024, aaddr + 1536, *LDSP(const v8s, baddr + 64), a1);
      a0 = mfma_tr(aaddr + 2048, aaddr + 2560, *LDSP(const v8s, baddr + 128), a0);
      a1 = mfma_tr(aaddr + 3072, aaddr + 3584, *LDSP(const v8s, baddr + 192), a1);
      q = a0 + a1;
    }
    if (hw != 0) {
      if (n < 2) *LDSP(v4f, ldsbase + L_QP + hw * 128 + (g * 2 + n) * 16) = q;
      LGKM0();
      if (lane == 0) *LDSP(volatile unsigned, ldsbase + L_QP + hw * 4) = 1u;
      if (WO_WHEN == 0) LOAD_WO();
    } else {
      for (unsigned spin = 0; spin < (SPIN_CAP << 6); ++spin) {
        const v4u f = *LDSP(volatile v4u, ldsbase + L_QP);
        if (f.y & f.z & f.w) break;
      }
      MEMFENCE();
      const unsigned qa = ldsbase + L_QP + (g * 2 + (n & 1)) * 16;
      q = (q + *LDSP(const v4f, qa + 128)) + (*LDSP(const v4f, qa + 256) + *LDSP(const v4f, qa + 384));
      if (n < 2) {
        const unsigned a = W_QG + (n * 4096 + 16 * sig + 4 * g) * 8;
        BSTORE16(gran2(rbf(q[0]), rbf(q[1]), T), a);
        BSTORE16(gran2(rbf(q[2]), rbf(q[3]), T), a + 16);
      }
      STAMP(3);
      const unsigned a = W_QG + (b * 4096 + kvh * 512) * 8 + lane * 16;
      v4u v[4];
      bool ok[4] = {false, false, false, false};
      for (unsigned spin = 0; spin < SPIN_CAP; ++spin) {
        MEMFENCE();
#pragma unroll
        for (int i = 0; i < 4; ++i) if (!ok[i]) v[i] = BLOAD16(a + i * 1024);
#pragma unroll
        for (int i = 0; i < 4; ++i) ok[i] = gok2(v[i], T);
        if (__all(ok[0] & ok[1] & ok[2] & ok[3])) break;
        __builtin_amdgcn_s_sleep(1);
      }
#pragma unroll
      for (int i = 0; i < 4; ++i) *LDSP(unsigned, ldsbase + L_QL + i * 256 + lane * 4) = ((v[i].x >> 8) & 0xffffu) | ((v[i].z << 8) & 0xffff0000u);
      if (lane == 0) *LDSP(v2u, ldsbase + L_MISC) = v2u{T.lo, T.hi};
    }
  }
  BARRIER();
  STAMP(4);
  if (WO_WHEN == 1 || (WO_WHEN == 0 && hw == 0)) LOAD_WO();
  float gate0 = 0.f, gate1 = 0.f, xr0 = 0.f, xr1 = 0.f;
  if (NO_MLP) {
  } else if (hw == 1) {
    if (wg < 224) {
      const int mat = wg >= 112, rem = wg - 112 * mat, cbk = rem >> 2, rbk = rem & 3;
      const char* src = (const char*)(mat ? w3 : w1) + (size_t)(128 * rbk + (lane >> 3)) * (FF * 2) + cbk * 128 + (lane & 7) * 16;
#pragma unroll
      for (int i = 0; i < 16; ++i) dma1(src + (size_t)i * (8 * FF * 2), ldsbase + L_STAGE + i * 1024);
      asm volatile("s_waitcnt vmcnt(0)" ::: "memory");
      const unsigned aaddr = ldsbase + L_STAGE + (lane >> 2) * 128 + (lane & 3) * 8, baddr = ldsbase + L_XN + (lane & 1) * 1024 + rbk * 256 + g * 16;
      v4f acc[4];
#pragma unroll
      for (int ct = 0; ct < 4; ++ct) acc[ct] = v4f{0, 0, 0, 0};
#pragma unroll
      for (int s = 0; s < 4; ++s) {
        const v8s bb = *LDSP(const v8s, baddr + s * 64);
#pragma unroll
        for (int ct = 0; ct < 4; ++ct) acc[ct] = mfma_tr(aaddr + s * 4096 + ct * 32, aaddr + s * 4096 + 2048 + ct * 32, bb, acc[ct]);
      }
      if (n < 2) {
        const unsigned a = W_HG + (((mat * 2 + n) * 4 + rbk) * FF + 64 * cbk + 4 * g) * 8;
#pragma unroll
        for (int ct = 0; ct < 4; ++ct) { BSTORE16(gran2(acc[ct][0], acc[ct][1], T), a + ct * 128); BSTORE16(gran2(acc[ct][2], acc[ct][3], T), a + ct * 128 + 16); }
      }
    }
  } else if (hw == 2) {
    const int rb = wg >> 2, cb = wg & 3;
    v4u w[7];
#pragma unroll
    for (int r = 0; r < 7; ++r) w[r] = __builtin_nontemporal_load((const GAS v4u*)(w2 + (size_t)(rb * 28 + g * 7 + r) * DM + cb * 128 + n * 8));
    const int t = lane < 56 ? lane : 0, tb = t / 28, tr = t % 28;
    const unsigned a = W_HG + (tb * 4 * FF + rb * 28 + tr) * 8;
    v2u pv[8];
    __builtin_amdgcn_s_sleep(127);
    for (unsigned spin = 0; spin < SPIN_CAP; ++spin) {
      __builtin_amdgcn_s_sleep(64);
      MEMFENCE();
      bool ok = true;
#pragma unroll
      for (int i = 0; i < 8; ++i) pv[i] = BLOAD8(a + ((i >> 2) * 8 + (i & 3)) * FF * 8);
#pragma unroll
      for (int i = 0; i < 8; ++i) ok &= gok1(pv[i], T);
      if (__all(ok)) break;
    }
    const float gt = rbf((gval(pv[0].x) + gval(pv[1].x)) + (gval(pv[2].x) + gval(pv[3].x))), up = rbf((gval(pv[4].x) + gval(pv[5].x)) + (gval(pv[6].x) + gval(pv[7].x)));
    const float hval = rbf(rbf(silu_f(gt)) * up);
    if (lane < 56) *LDSP(float, ldsbase + L_H + (tb * 32 + (tr / 7) * 8 + tr % 7) * 4) = hval;
    LGKM0();
#pragma unroll
    for (int bb = 0; bb < 2; ++bb) {
      const v4f h0 = *LDSP(const v4f, ldsbase + L_H + (bb * 32 + g * 8) * 4), h1 = *LDSP(const v4f, ldsbase + L_H + (bb * 32 + g * 8 + 4) * 4);
      const float hh[7] = {h0[0], h0[1], h0[2], h0[3], h1[0], h1[1], h1[2]};
      float z[8];
#pragma unroll
      for (int c = 0; c < 8; ++c) z[c] = 0.f;
#pragma unroll
      for (int r = 0; r < 7; ++r)
#pragma unroll
        for (int j = 0; j < 4; ++j) { z[2 * j] = fmaf(hh[r], lo16(w[r][j]), z[2 * j]); z[2 * j + 1] = fmaf(hh[r], hi16(w[r][j]), z[2 * j + 1]); }
      float t4[4];
#pragma unroll
      for (int j = 0; j < 4; ++j) t4[j] = swap32_add(z[j], z[j + 4]);
      BSTORE16(gran2(swap16_add(t4[0], t4[2]), swap16_add(t4[1], t4[3]), T), W_INZ + ((bb * 128 + 32 * cb + 2 * n + (g >> 1)) * 64 + rb) * 32 + (g & 1) * 16);
    }
  } else if (hw == 3) {
    if (wg == 0) {
      for (unsigned spin = 0; spin < SPIN_CAP; ++spin) {
        __builtin_amdgcn_s_sleep(32);
        MEMFENCE();
        bool ok = true;
#pragma unroll
        for (int i = 0; i < 4; ++i) ok &= gok1(BLOAD8(W_QG + (i * 64 + lane) * 128), T);
        if (__all(ok)) break;
      }
      if (lane == 0) {
        const v2u e = BLOAD8(W_EPOCH);
        const u64 tv = (((u64)e.y << 32) | e.x) + 1;
        __builtin_amdgcn_raw_buffer_store_b64(v2u{(unsigned)tv, (unsigned)(tv >> 32)}, rs, W_EPOCH, 0, 16);
      }
    }
    const unsigned xv = __builtin_nontemporal_load((const GAS unsigned*)(x + wg * 4 + (lane & 1) * 2));
    xr0 = lo16(xv); xr1 = hi16(xv);
    const unsigned a = W_INZ + wg * 2048 + lane * 16;
    v4u v[2];
    __builtin_amdgcn_s_sleep(127); __builtin_amdgcn_s_sleep(127); __builtin_amdgcn_s_sleep(127);
    for (unsigned spin = 0; spin < SPIN_CAP; ++spin) {
      __builtin_amdgcn_s_sleep(64);
      MEMFENCE();
      v[0] = BLOAD16(a); v[1] = BLOAD16(a + 1024);
      if (__all(gok2(v[0], T) & gok2(v[1], T))) break;
    }
    const float z0 = parity_sum(gval(v[0].x) + gval(v[1].x)), z1 = parity_sum(gval(v[0].z) + gval(v[1].z));
    gate0 = rbf(silu_f(rbf(z0))); gate1 = rbf(silu_f(rbf(z1)));
  }
  if (hw != 0) STAMP(1);
  if (WO_WHEN == 2) LOAD_WO();
  if (hw != 0) STAMP(2);
  BARRIER();
  STAMP(5);
  {
    unsigned op[4];
    if (hw == 0) {
      const unsigned a = W_IN1 + (wg * 16 + n) * 320;
      v4u v[4], ml;
      bool ok = false;
      for (unsigned spin = 0; spin < SPIN_CAP; ++spin) {
        MEMFENCE();
        if (!ok) {
#pragma unroll
          for (int i = 0; i < 4; ++i) v[i] = BLOAD16(a + g * 64 + i * 16);
          ml = BLOAD16(a + 256);
          ok = gok2(v[0], T) & gok2(v[1], T) & gok2(v[2], T) & gok2(v[3], T) & gok2(ml, T);
        }
        if (__all(ok)) break;
        if (POLL_SLEEP) __builtin_amdgcn_s_sleep(POLL_SLEEP);
      }
      STAMP(6);
      const float mp = gval(ml.x), lp = gval(ml.z);
      const float M = row_max(mp);
      const float wt = __builtin_amdgcn_exp2f((mp - M) * LOG2E);
      const float invL = __builtin_amdgcn_rcpf(row_sum(wt * lp));
#pragma unroll
      for (int i = 0; i < 4; ++i) op[i] = pk2(row_sum(wt * gval(v[i].x)) * invL, row_sum(wt * gval(v[i].z)) * invL);
      if (n == 0) *LDSP(v4u, ldsbase + L_OP + g * 16) = v4u{op[0], op[1], op[2], op[3]};
      LGKM0();
      if (lane == 0) *LDSP(volatile unsigned, ldsbase + L_OP + 64) = 1u;
    } else {
      for (unsigned spin = 0; spin < (SPIN_CAP << 8); ++spin) {
        if (*LDSP(volatile unsigned, ldsbase + L_OP + 64)) break;
        __builtin_amdgcn_s_sleep(1);
      }
      MEMFENCE();
      const v4u o4 = *LDSP(const v4u, ldsbase + L_OP + g * 16);
      op[0] = o4.x; op[1] = o4.y; op[2] = o4.z; op[3] = o4.w;
      STAMP(6);
    }
    float y[8];
#pragma unroll
    for (int c = 0; c < 8; ++c) { float acc = 0.f;
#pragma unroll
      for (int k = 0; k < 4; ++k) acc = dot2(wp[k][c], op[k], acc);
      y[c] = acc; }
    float t4[4];
#pragma unroll
    for (int j = 0; j < 4; ++j) t4[j] = swap32_add(y[j], y[j + 4]);
    const unsigned a2 = W_INY + ((b * 128 + 32 * hw + 2 * n + (g >> 1)) * 128 + (wg & 127)) * 32 + (g & 1) * 16;
    BSTORE16(gran2(swap16_add(t4[0], t4[2]), swap16_add(t4[1], t4[3]), T), a2);
  }
  if (hw == 3) {
    const unsigned a = W_INY + wg * 4096 + lane * 16;
    v4u v[4];
    bool ok[4] = {false, false, false, false};
    for (unsigned spin = 0; spin < SPIN_CAP; ++spin) {
      MEMFENCE();
#pragma unroll
      for (int i = 0; i < 4; ++i) if (!ok[i]) v[i] = BLOAD16(a + i * 1024);
#pragma unroll
      for (int i = 0; i < 4; ++i) ok[i] = gok2(v[i], T);
      if (__all(ok[0] & ok[1] & ok[2] & ok[3])) break;
      if (POLL_SLEEP) __builtin_amdgcn_s_sleep(POLL_SLEEP);
    }
    const float s0 = parity_sum((gval(v[0].x) + gval(v[1].x)) + (gval(v[2].x) + gval(v[3].x)));
    const float s1 = parity_sum((gval(v[0].z) + gval(v[1].z)) + (gval(v[2].z) + gval(v[3].z)));
    if (lane < 2) *(GAS unsigned*)(out + wg * 4 + lane * 2) = pk2(xr0 + rbf(rbf(s0) * gate0), xr1 + rbf(rbf(s1) * gate1));
  }
  STAMP(7); STAMP_FLUSH();
}

extern "C" void kernel_launch(void* const* d_in, const int* in_sizes, int n_in, void* d_out, int out_size, void* d_ws, size_t ws_size, hipStream_t stream) {
  (void)hipFuncSetAttribute((const void*)k_fused, hipFuncAttributeMaxDynamicSharedMemorySize, LDS_BYTES);
  k_fused<<<NWG, 512, LDS_BYTES, stream>>>((const u16*)d_in[0], (const u16*)d_in[1], (const u16*)d_in[2], (const u16*)d_in[3], (const u16*)d_in[4],
                                           (const u16*)d_in[6], (const u16*)d_in[7], (const u16*)d_in[8], (u16*)d_out, (unsigned char*)d_ws);
}
```

```cpp
#define PROBE_WAVE 4
#define PROBE_IDX 3
#include <hip/hip_runtime.h>
#include <hip/hip_bf16.h>
#include <stdint.h>

#define GAS __attribute__((address_space(1)))
#define LAS __attribute__((address_space(3)))
typedef unsigned v4u __attribute__((ext_vector_type(4)));
typedef unsigned v2u __attribute__((ext_vector_type(2)));
typedef float v4f __attribute__((ext_vector_type(4)));
typedef float v2f __attribute__((ext_vector_type(2)));
typedef __bf16 v8bf __attribute__((ext_vector_type(8)));
typedef __bf16 v2bf __attribute__((ext_vector_type(2)));
typedef short v4s __attribute__((ext_vector_type(4)));
typedef short v8s __attribute__((ext_vector_type(8)));
typedef unsigned short u16;
typedef unsigned long long u64;

constexpr int DM = 512, SK = 32768, FF = 1792;
constexpr int NWG = 256, NSPLIT = 16;
constexpr float SCALE = 0.088388347648318440f, LOG2E = 1.4426950408889634f;
constexpr int SW = 4, RSLOT = 4;
#define NSTAT 24
#define KPOL " nt"
#define VPOL " nt"
#define POLK_EXPR false
#define POLV_EXPR false
#define DEPTH 2
#define NO_MLP 0
#define NO_STREAM 0
#define NO_SWZ 0
#define POLL_SLEEP 0
#define WO_WHEN 2
constexpr int NT_CLASS = SK / 16 / SW, DYN0 = NSTAT * NSPLIT;

constexpr int L_RING = 0, L_STAGE = SW * RSLOT * 8192, L_XN = L_STAGE + 16384, L_QL = L_XN + 2048, L_MISC = L_QL + 1024, L_H = L_MISC + 256, L_OP = L_H + 256, L_QP = L_OP + 256,
              LDS_BYTES = L_QP + 512;
constexpr unsigned W_EPOCH = 0, W_CTR = 4096, W_QG = W_CTR + 64 * 256, W_HG = W_QG + 2 * 4096 * 8, W_IN1 = W_HG + 16 * FF * 8, W_INY = W_IN1 + NWG * 16 * 320, W_INZ = W_INY + NWG * 4096,
                   W_END = W_INZ + NWG * 2048;
constexpr unsigned SPIN_CAP = 1u << 16;

__device__ __forceinline__ float bf2f(unsigned v) { return __uint_as_float(v << 16); }
__device__ __forceinline__ u16 f2bf(float f) { __bf16 b = (__bf16)f; return __builtin_bit_cast(u16, b); }
__device__ __forceinline__ float rbf(float f) { return bf2f(f2bf(f)); }
__device__ __forceinline__ unsigned pk2(float lo, float hi) { v2bf p = {(__bf16)lo, (__bf16)hi}; return __builtin_bit_cast(unsigned, p); }
__device__ __forceinline__ float lo16(unsigned v) { return __uint_as_float(v << 16); }
__device__ __forceinline__ float hi16(unsigned v) { return __uint_as_float(v & 0xffff0000u); }
template <int CTRL> __device__ __forceinline__ float dpp(float x) {
  return __builtin_bit_cast(float, __builtin_amdgcn_mov_dpp(__builtin_bit_cast(int, x), CTRL, 0xf, 0xf, true));
}
constexpr int XOR1 = 0xB1, XOR2 = 0x4E, HMIR = 0x141, MIR = 0x140, ROR2 = 0x122, ROR4 = 0x124, ROR8 = 0x128;
__device__ __forceinline__ float swap32_add(float a, float b) { auto s = __builtin_amdgcn_permlane32_swap(__float_as_uint(a), __float_as_uint(b), false, false); return __uint_as_float(s[0]) + __uint_as_float(s[1]); }
__device__ __forceinline__ float swap16_add(float a, float b) { auto s = __builtin_amdgcn_permlane16_swap(__float_as_uint(a), __float_as_uint(b), false, false); return __uint_as_float(s[0]) + __uint_as_float(s[1]); }
__device__ __forceinline__ float xrow16_max(float x) {
  auto s = __builtin_amdgcn_permlane16_swap(__float_as_uint(x), __float_as_uint(x), false, false);
  x = fmaxf(__uint_as_float(s[0]), __uint_as_float(s[1]));
  auto t = __builtin_amdgcn_permlane32_swap(__float_as_uint(x), __float_as_uint(x), false, false);
  return fmaxf(__uint_as_float(t[0]), __uint_as_float(t[1]));
}
__device__ __forceinline__ float xrow16_sum(float x) { x = swap16_add(x, x); return swap32_add(x, x); }
__device__ __forceinline__ float row_sum(float x) { x += dpp<XOR1>(x); x += dpp<XOR2>(x); x += dpp<HMIR>(x); x += dpp<MIR>(x); return x; }
__device__ __forceinline__ float row_max(float x) { x = fmaxf(x, dpp<XOR1>(x)); x = fmaxf(x, dpp<XOR2>(x)); x = fmaxf(x, dpp<HMIR>(x)); x = fmaxf(x, dpp<MIR>(x)); return x; }
__device__ __forceinline__ float wave_sum(float x) { return xrow16_sum(row_sum(x)); }
__device__ __forceinline__ float parity_sum(float x) { x += dpp<ROR2>(x); x += dpp<ROR4>(x); x += dpp<ROR8>(x); return xrow16_sum(x); }
__device__ __forceinline__ float silu_f(float x) { return x * __builtin_amdgcn_rcpf(1.f + __builtin_amdgcn_exp2f(-x * LOG2E)); }
__device__ __forceinline__ float dot2(unsigned a, unsigned b, float c) { return __builtin_amdgcn_fdot2_f32_bf16(__builtin_bit_cast(v2bf, a), __builtin_bit_cast(v2bf, b), c, false); }

#define STAMP(i) do { if ((i) == 0) tstart = __builtin_amdgcn_s_memrealtime(); if (wave == PROBE_WAVE && (i) == PROBE_IDX && lane == 0) *LDSP(volatile u64, ldsbase + L_MISC + 64) = __builtin_amdgcn_s_memrealtime(); \
    if (wave == 7 && (i) == 7) { const u64 now_ = __builtin_amdgcn_s_memrealtime(); asm volatile("s_waitcnt lgkmcnt(0)" ::: "memory"); const u64 d_ = *LDSP(volatile u64, ldsbase + L_MISC + 64) - tstart; \
      const u64 until_ = now_ + 4 * (d_ < 20000 ? d_ : 20000); while (__builtin_amdgcn_s_memrealtime() < until_) __builtin_amdgcn_s_sleep(2); } } while (0)
#define STAMP_DECL u64 tstart = 0
#define STAMP_FLUSH() do {} while (0)

struct Tag { unsigned lo, hi; };
__device__ __forceinline__ v4u gran2(float a, float b, Tag t) { return v4u{(__float_as_uint(a) >> 8) | t.lo, t.hi, (__float_as_uint(b) >> 8) | t.lo, t.hi}; }
__device__ __forceinline__ bool gok2(v4u v, Tag t) { return (v.y == t.hi) & (v.w == t.hi) & (((v.x ^ t.lo) | (v.z ^ t.lo)) < 0x1000000u); }
__device__ __forceinline__ bool gok1(v2u v, Tag t) { return (v.y == t.hi) & ((v.x ^ t.lo) < 0x1000000u); }
__device__ __forceinline__ float gval(unsigned lo) { return __uint_as_float(lo << 8); }
#define BLOAD16(off) __builtin_amdgcn_raw_buffer_load_b128(rs, (off), 0, 16)
#define BLOAD8(off) __builtin_amdgcn_raw_buffer_load_b64(rs, (off), 0, 16)
#define BSTORE16(v, off) __builtin_amdgcn_raw_buffer_store_b128((v), rs, (off), 0, 16)
#define MEMFENCE() asm volatile("" ::: "memory")
#define LGKM0() asm volatile("s_waitcnt lgkmcnt(0)" ::: "memory")
#define BARRIER() do { LGKM0(); __builtin_amdgcn_s_barrier(); MEMFENCE(); } while (0)
#define LDSP(T, a) ((LAS T*)(uintptr_t)(a))

#define DMA_HALF(POL, oe, oo, gb, ldsaddr)                                                       \
  asm volatile("s_mov_b32 %0, m0\n\t"                                                           \
               "s_mov_b32 m0, %4\n\t"                                                           \
               "s_nop 0\n\t"                                                                    \
               "global_load_lds_dwordx4 %1, %3" POL "\n\t"                                      \
               "global_load_lds_dwordx4 %2, %3 offset:1024" POL "\n\t"                          \
               "global_load_lds_dwordx4 %1, %3 offset:2048" POL "\n\t"                          \
               "global_load_lds_dwordx4 %2, %3 offset:3072" POL "\n\t"                          \
               "s_mov_b32 m0, %0"                                                               \
               : "=&s"(m0keep) : "v"(oe), "v"(oo), "s"(gb), "s"(ldsaddr) : "memory")
#define DMA_TILE(kb, vb, ldsaddr) do { const unsigned l2_ = (ldsaddr) + 4096;                     \
    if (polk) DMA_HALF("", koe, koo, kb, ldsaddr); else DMA_HALF(" nt", koe, koo, kb, ldsaddr);   \
    if (polv) DMA_HALF("", voe, voo, vb, l2_); else DMA_HALF(" nt", voe, voo, vb, l2_); } while (0)

__device__ __forceinline__ void dma1(const void* gsrc, unsigned lds_dst) {
  unsigned keep;
  asm volatile("s_mov_b32 %0, m0\n\ts_mov_b32 m0, %2\n\ts_nop 0\n\tglobal_load_lds_dwordx4 %1, off nt\n\ts_mov_b32 m0, %0" : "=&s"(keep) : "v"(gsrc), "s"(lds_dst) : "memory");
}
__device__ __forceinline__ v4f mfma_tr(unsigned a_lo, unsigned a_hi, v8s bb, v4f acc) {
  const v4s a0 = __builtin_amdgcn_ds_read_tr16_b64_v4i16(LDSP(v4s, a_lo));
  const v4s a1 = __builtin_amdgcn_ds_read_tr16_b64_v4i16(LDSP(v4s, a_hi));
  const v8s a = __builtin_shufflevector(a0, a1, 0, 1, 2, 3, 4, 5, 6, 7);
  return __builtin_amdgcn_mfma_f32_16x16x32_bf16(__builtin_bit_cast(v8bf, a), __builtin_bit_cast(v8bf, bb), acc, 0, 0, 0);
}

__global__ void __launch_bounds__(512, 2) k_fused(const u16* __restrict__ x, const u16* __restrict__ Kc, const u16* __restrict__ Vc, const u16* __restrict__ wq,
                                                  const u16* __restrict__ wo, const u16* __restrict__ w1, const u16* __restrict__ w3, const u16* __restrict__ w2,
                                                  u16* __restrict__ out, unsigned char* __restrict__ ws) {
  extern __shared__ __attribute__((aligned(16))) unsigned char lds[];
  const int tid = threadIdx.x, lane = tid & 63, wave = __builtin_amdgcn_readfirstlane(tid >> 6);
  const int wg = blockIdx.x, grp = wg >> 4, sp = wg & 15, b = grp >> 3, kvh = grp & 7;
  const unsigned ldsbase = (unsigned)(uintptr_t)(LAS unsigned char*)lds;
  const __amdgpu_buffer_rsrc_t rs = __builtin_amdgcn_make_buffer_rsrc(ws, 0, W_END, 0x00020000);
  const int n = lane & 15, g = lane >> 4;
  STAMP_DECL; STAMP(0);

  if (wave < SW) {
    const unsigned ring = ldsbase + L_RING + wave * (RSLOT * 8192);
    const unsigned r4 = lane >> 4, pos = lane & 15;
    const unsigned koe = NO_SWZ ? lane * 16 : r4 * 256 + ((pos & 8) | ((pos & 7) ^ r4)) * 16;
    const unsigned koo = NO_SWZ ? lane * 16 : r4 * 256 + ((pos & 8) | ((pos & 7) ^ (4 + r4))) * 16;
    const unsigned voe = NO_SWZ ? lane * 16 : r4 * 256 + (pos ^ (r4 << 1)) * 16;
    const unsigned voo = NO_SWZ ? lane * 16 : r4 * 256 + (pos ^ ((4 + r4) << 1)) * 16;
    const size_t gbase = (size_t)grp * SK * 256 + (size_t)wave * 4096;
    const int t0s = sp * NSTAT;
    const GAS unsigned* ctr = (const GAS unsigned*)(ws + W_CTR + (grp * SW + wave) * 256);
    const unsigned one = 1;
    const char* kptr = (const char*)Kc + gbase;
    const char* vptr = (const char*)Vc + gbase;
    unsigned m0keep;
#define ISSUE(t, slot) do { if (NO_STREAM) break; const int tt_ = (t); const bool polk = POLK_EXPR, polv = POLV_EXPR; const char* k0_ = kptr + (size_t)(t) * (SW * 4096); const char* v0_ = vptr + (size_t)(t) * (SW * 4096); const unsigned l_ = ring + (slot) * 8192; DMA_TILE(k0_, v0_, l_); } while (0)
#define TICKET(par) do { if (par) asm volatile("s_mov_b64 exec, 1\n\tglobal_atomic_add v251, %0, %1, off sc0\n\ts_mov_b64 exec, -1" :: "v"(ctr), "v"(one) : "memory", "v251"); \
                        else asm volatile("s_mov_b64 exec, 1\n\tglobal_atomic_add v250, %0, %1, off sc0\n\ts_mov_b64 exec, -1" :: "v"(ctr), "v"(one) : "memory", "v250"); } while (0)
    __builtin_amdgcn_s_barrier();
    ISSUE(t0s, 0); ISSUE(t0s + 1, 1);
    if (DEPTH == 3) { ISSUE(t0s + 2, 2); asm volatile("s_waitcnt vmcnt(16)" ::: "memory"); STAMP(1); ISSUE(t0s + 3, 3); }
    else {
      asm volatile("s_waitcnt vmcnt(8)" ::: "memory");
      STAMP(1);
      ISSUE(t0s + 2, 2);
      asm volatile("s_waitcnt vmcnt(8)" ::: "memory");
      ISSUE(t0s + 3, 3);
    }
    unsigned ka[4], va[8];
#pragma unroll
    for (int t = 0; t < 4; ++t) ka[t] = ring + n * 256 + ((8 * (g & 1)) | ((4 * (g >> 1) + t) ^ (n & 7))) * 16;
    { const unsigned q4 = (lane >> 2) & 3, p = lane & 3, row = 4 * g + q4;
#pragma unroll
      for (int dt = 0; dt < 8; ++dt) va[dt] = ring + 4096 + row * 256 + ((dt ^ (row & 7)) << 5) + p * 8; }
    STAMP(2);
    BARRIER();
    STAMP(3);
    v8bf qf[4];
#pragma unroll
    for (int t = 0; t < 4; ++t) qf[t] = __builtin_bit_cast(v8bf, *LDSP(const v4u, ldsbase + L_QL + (n & 3) * 256 + (8 * (g & 1) + 4 * (g >> 1) + t) * 16));
    const v2u tg = *LDSP(const v2u, ldsbase + L_MISC);
    const Tag T{tg.x, tg.y};
    float m = -INFINITY, l = 0.f;
    v4f O[8];
#pragma unroll
    for (int dt = 0; dt < 8; ++dt) O[dt] = v4f{0, 0, 0, 0};
    int i = 0, rem = NO_STREAM ? 0 : 3; bool live = !NO_STREAM;
    for (;;) {
      bool done = false;
#pragma unroll
      for (int u = 0; u < 4; ++u) {
        if (done) break;
        if (i >= RSLOT - DEPTH) {
          if (rem >= 2 && DEPTH == 3) asm volatile("s_waitcnt vmcnt(16)" ::: "memory");
          else if (rem >= 1) asm volatile("s_waitcnt vmcnt(8)" ::: "memory");
          else asm volatile("s_waitcnt vmcnt(0)" ::: "memory");
          if (live) {
            int t = t0s + i + DEPTH;
            if (i + DEPTH >= NSTAT) {
              int tk = NT_CLASS;
              if (DYN0 < NT_CLASS) { if ((u - (DEPTH - 1)) & 1) asm volatile("v_readfirstlane_b32 %0, v251" : "=s"(tk) :: "memory"); else asm volatile("v_readfirstlane_b32 %0, v250" : "=s"(tk) :: "memory"); }
              t = DYN0 + tk;
              live = (unsigned)tk < (unsigned)(NT_CLASS - DYN0);
            }
            if (live) {
              if (i + 2 * DEPTH - 1 >= NSTAT && DYN0 < NT_CLASS) TICKET(u & 1);
              ISSUE(t, (u + DEPTH) & 3);
              ++rem;
            }
          }
        }
        const unsigned so = u * 8192;
        v4u kf[4]; v4s vf[8];
#pragma unroll
        for (int t = 0; t < 4; ++t) kf[t] = *LDSP(const v4u, ka[t] + so);
#pragma unroll
        for (int dt = 0; dt < 8; ++dt) vf[dt] = __builtin_amdgcn_ds_read_tr16_b64_v4i16(LDSP(v4s, va[dt] + so));
        v4f S = {0, 0, 0, 0};
#pragma unroll
        for (int t = 0; t < 4; ++t) S = __builtin_amdgcn_mfma_f32_16x16x32_bf16(__builtin_bit_cast(v8bf, kf[t]), qf[t], S, 0, 0, 0);
        const unsigned a01 = pk2(S[0], S[1]), a23 = pk2(S[2], S[3]);
        const unsigned c01 = pk2(lo16(a01) * SCALE, hi16(a01) * SCALE), c23 = pk2(lo16(a23) * SCALE, hi16(a23) * SCALE);
        const float s0 = lo16(c01), s1 = hi16(c01), s2 = lo16(c23), s3 = hi16(c23);
        const float tm = xrow16_max(fmaxf(fmaxf(s0, s1), fmaxf(s2, s3)));
        if (__any(tm > m)) {
          const float mn = fmaxf(m, tm);
          const float alpha = __builtin_amdgcn_exp2f((m - mn) * LOG2E);
          l *= alpha;
#pragma unroll
          for (int dt = 0; dt < 8; ++dt) O[dt] *= alpha;
          m = mn;
        }
        const float p0 = __builtin_amdgcn_exp2f((s0 - m) * LOG2E), p1 = __builtin_amdgcn_exp2f((s1 - m) * LOG2E);
        const float p2 = __builtin_amdgcn_exp2f((s2 - m) * LOG2E), p3 = __builtin_amdgcn_exp2f((s3 - m) * LOG2E);
        l += (p0 + p1) + (p2 + p3);
        const v2u pp = {pk2(p0, p1), pk2(p2, p3)};
        const v4s pf = __builtin_bit_cast(v4s, pp);
#pragma unroll
        for (int dt = 0; dt < 8; ++dt) O[dt] = __builtin_amdgcn_mfma_f32_16x16x16bf16_1k(vf[dt], pf, O[dt], 0, 0, 0);
        LGKM0();
        if (rem == 0) done = true;
        --rem; ++i;
      }
      if (done) break;
    }
    STAMP(4);
    l = xrow16_sum(l);
    if (n < 4) {
      const unsigned r = ring + n * 528;
#pragma unroll
      for (int dt = 0; dt < 8; ++dt) *LDSP(v4f, r + 16 + (16 * dt + 4 * g) * 4) = O[dt];
      if (g == 0) *LDSP(v2f, r) = v2f{m, l};
    }
    BARRIER();
    STAMP(5);
    {
      const int h = wave;
      v2f ml[4], ov[4];
#pragma unroll
      for (int w = 0; w < 4; ++w) { const unsigned r = ldsbase + L_RING + w * (RSLOT * 8192) + h * 528; ml[w] = *LDSP(const v2f, r); ov[w] = *LDSP(const v2f, r + 16 + lane * 8); }
      const float M = fmaxf(fmaxf(ml[0].x, ml[1].x), fmaxf(ml[2].x, ml[3].x));
      float L = 0, o0 = 0, o1 = 0;
#pragma unroll
      for (int w = 0; w < 4; ++w) { const float wt = __builtin_amdgcn_exp2f((ml[w].x - M) * LOG2E); L = fmaf(wt, ml[w].y, L); o0 = fmaf(wt, ov[w].x, o0); o1 = fmaf(wt, ov[w].y, o1); }
      const unsigned rec = W_IN1 + ((grp * 16 + 4 * h + g) * 16 + sp) * 320;
      BSTORE16(gran2(o0, o1, T), rec + n * 16);
      if (n == 0) BSTORE16(gran2(M, L, T), rec + 256);
    }
    STAMP(6); STAMP_FLUSH();
    return;
  }

  const int hw = wave - SW;
  Tag T;
  unsigned wp[4][8];
#define LOAD_WO() do { const int row0 = (kvh * 4 + (sp >> 2)) * 128 + (sp & 3) * 32 + 8 * g; v4u w_[8];                                                              \
    _Pragma("unroll") for (int i = 0; i < 8; ++i) w_[i] = __builtin_nontemporal_load((const GAS v4u*)(wo + (size_t)(row0 + i) * DM + 128 * hw + 8 * n));              \
    _Pragma("unroll") for (int k = 0; k < 4; ++k) _Pragma("unroll") for (int j = 0; j < 4; ++j) {                                                                   \
      wp[k][2 * j] = __builtin_amdgcn_perm(w_[2 * k + 1][j], w_[2 * k][j], 0x05040100u); wp[k][2 * j + 1] = __builtin_amdgcn_perm(w_[2 * k + 1][j], w_[2 * k][j], 0x07060302u); } \
    _Pragma("unroll") for (int k = 0; k < 4; ++k) _Pragma("unroll") for (int j = 0; j < 8; ++j) asm volatile("" : "+v"(wp[k][j])); } while (0)
  {
    const int sig = (wg & 7) * 32 + (wg >> 3);
    v4u x0, x1; v2u ep;
    { const u16* xp = x + lane * 8; const unsigned char* e = ws + W_EPOCH;
      asm volatile("global_load_dwordx4 %0, %3, off nt\n\tglobal_load_dwordx4 %1, %3, off offset:1024 nt\n\tglobal_load_dwordx2 %2, %4, off nt" : "=&v"(x0), "=&v"(x1), "=&v"(ep) : "v"(xp), "v"(e) : "memory"); }
    { const char* src = (const char*)wq + (size_t)(128 * hw + (lane >> 1)) * 8192 + sig * 32 + (lane & 1) * 16;
#pragma unroll
      for (int i = 0; i < 4; ++i) dma1(src + (size_t)i * (32 * 8192), ldsbase + L_STAGE + (4 * hw + i) * 1024); }
    if (hw == 0 && lane == 0) { *LDSP(v4u, ldsbase + L_QP) = v4u{0, 0, 0, 0}; *LDSP(unsigned, ldsbase + L_OP + 64) = 0u; }
    BARRIER();
    asm volatile("s_waitcnt vmcnt(4)" : "+v"(x0), "+v"(x1), "+v"(ep) :: "memory");
    STAMP(1);
    if (hw == 0 && wg < 16 && lane < SW)
      __builtin_amdgcn_raw_buffer_store_b32(0u, rs, W_CTR + (((wg >> 3) * 8 + (wg & 7)) * SW + lane) * 256, 0, 16);
    const u64 tv = (((u64)ep.y << 32) | ep.x) + 1;
    T = Tag{(unsigned)(tv & 0xff) << 24, (unsigned)(tv >> 8)};
    {
      float v[2][8];
#pragma unroll
      for (int j = 0; j < 4; ++j) { v[0][2 * j] = lo16(x0[j]); v[0][2 * j + 1] = hi16(x0[j]); v[1][2 * j] = lo16(x1[j]); v[1][2 * j + 1] = hi16(x1[j]); }
#pragma unroll
      for (int bb = 0; bb < 2; ++bb) {
        const float s = ((v[bb][0] + v[bb][1]) + (v[bb][2] + v[bb][3])) + ((v[bb][4] + v[bb][5]) + (v[bb][6] + v[bb][7]));
        const float mean = wave_sum(s) * (1.f / DM);
        float q = 0;
#pragma unroll
        for (int j = 0; j < 8; ++j) { const float d = v[bb][j] - mean; q = fmaf(d, d, q); }
        const float var = wave_sum(q) * (1.f / DM);
        const float mb = rbf(mean);
        const float den = rbf(__builtin_amdgcn_sqrtf(rbf(rbf(var) + 1e-5f)));
        const float inv = __builtin_amdgcn_rcpf(den);
        v4u o;
#pragma unroll
        for (int j = 0; j < 4; ++j) o[j] = pk2(rbf(v[bb][2 * j] - mb) * inv, rbf(v[bb][2 * j + 1] - mb) * inv);
        const unsigned xa = ldsbase + L_XN + bb * 1024 + (lane >> 2) * 64 + (lane & 1) * 32 + ((lane >> 1) & 1) * 8;
        if (g == hw) { *LDSP(v2u, xa) = v2u{o[0], o[1]}; *LDSP(v2u, xa + 16) = v2u{o[2], o[3]}; }
      }
    }
    asm volatile("s_waitcnt vmcnt(0) lgkmcnt(0)" ::: "memory");
    STAMP(2);
    v4f q;
    {
      const unsigned aaddr = ldsbase + L_STAGE + hw * 4096 + lane * 8, baddr = ldsbase + L_XN + (lane & 1) * 1024 + hw * 256 + g * 16;
      v4f a0 = {0, 0, 0, 0}, a1 = {0, 0, 0, 0};
      a0 = mfma_tr(aaddr, aaddr + 512, *LDSP(const v8s, baddr), a0);
      a1 = mfma_tr(aaddr + 1024, aaddr + 1536, *LDSP(const v8s, baddr + 64), a1);
      a0 = mfma_tr(aaddr + 2048, aaddr + 2560, *LDSP(const v8s, baddr + 128), a0);
      a1 = mfma_tr(aaddr + 3072, aaddr + 3584, *LDSP(const v8s, baddr + 192), a1);
      q = a0 + a1;
    }
    if (hw != 0) {
      if (n < 2) *LDSP(v4f, ldsbase + L_QP + hw * 128 + (g * 2 + n) * 16) = q;
      LGKM0();
      if (lane == 0) *LDSP(volatile unsigned, ldsbase + L_QP + hw * 4) = 1u;
      if (WO_WHEN == 0) LOAD_WO();
    } else {
      for (unsigned spin = 0; spin < (SPIN_CAP << 6); ++spin) {
        const v4u f = *LDSP(volatile v4u, ldsbase + L_QP);
        if (f.y & f.z & f.w) break;
      }
      MEMFENCE();
      const unsigned qa = ldsbase + L_QP + (g * 2 + (n & 1)) * 16;
      q = (q + *LDSP(const v4f, qa + 128)) + (*LDSP(const v4f, qa + 256) + *LDSP(const v4f, qa + 384));
      if (n < 2) {
        const unsigned a = W_QG + (n * 4096 + 16 * sig + 4 * g) * 8;
        BSTORE16(gran2(rbf(q[0]), rbf(q[1]), T), a);
        BSTORE16(gran2(rbf(q[2]), rbf(q[3]), T), a + 16);
      }
      STAMP(3);
      const unsigned a = W_QG + (b * 4096 + kvh * 512) * 8 + lane * 16;
      v4u v[4];
      bool ok[4] = {false, false, false, false};
      for (unsigned spin = 0; spin < SPIN_CAP; ++spin) {
        MEMFENCE();
#pragma unroll
        for (int i = 0; i < 4; ++i) if (!ok[i]) v[i] = BLOAD16(a + i * 1024);
#pragma unroll
        for (int i = 0; i < 4; ++i) ok[i] = gok2(v[i], T);
        if (__all(ok[0] & ok[1] & ok[2] & ok[3])) break;
        __builtin_amdgcn_s_sleep(1);
      }
#pragma unroll
      for (int i = 0; i < 4; ++i) *LDSP(unsigned, ldsbase + L_QL + i * 256 + lane * 4) = ((v[i].x >> 8) & 0xffffu) | ((v[i].z << 8) & 0xffff0000u);
      if (lane == 0) *LDSP(v2u, ldsbase + L_MISC) = v2u{T.lo, T.hi};
    }
  }
  BARRIER();
  STAMP(4);
  if (WO_WHEN == 1 || (WO_WHEN == 0 && hw == 0)) LOAD_WO();
  float gate0 = 0.f, gate1 = 0.f, xr0 = 0.f, xr1 = 0.f;
  if (NO_MLP) {
  } else if (hw == 1) {
    if (wg < 224) {
      const int mat = wg >= 112, rem = wg - 112 * mat, cbk = rem >> 2, rbk = rem & 3;
      const char* src = (const char*)(mat ? w3 : w1) + (size_t)(128 * rbk + (lane >> 3)) * (FF * 2) + cbk * 128 + (lane & 7) * 16;
#pragma unroll
      for (int i = 0; i < 16; ++i) dma1(src + (size_t)i * (8 * FF * 2), ldsbase + L_STAGE + i * 1024);
      asm volatile("s_waitcnt vmcnt(0)" ::: "memory");
      const unsigned aaddr = ldsbase + L_STAGE + (lane >> 2) * 128 + (lane & 3) * 8, baddr = ldsbase + L_XN + (lane & 1) * 1024 + rbk * 256 + g * 16;
      v4f acc[4];
#pragma unroll
      for (int ct = 0; ct < 4; ++ct) acc[ct] = v4f{0, 0, 0, 0};
#pragma unroll
      for (int s = 0; s < 4; ++s) {
        const v8s bb = *LDSP(const v8s, baddr + s * 64);
#pragma unroll
        for (int ct = 0; ct < 4; ++ct) acc[ct] = mfma_tr(aaddr + s * 4096 + ct * 32, aaddr + s * 4096 + 2048 + ct * 32, bb, acc[ct]);
      }
      if (n < 2) {
        const unsigned a = W_HG + (((mat * 2 + n) * 4 + rbk) * FF + 64 * cbk + 4 * g) * 8;
#pragma unroll
        for (int ct = 0; ct < 4; ++ct) { BSTORE16(gran2(acc[ct][0], acc[ct][1], T), a + ct * 128); BSTORE16(gran2(acc[ct][2], acc[ct][3], T), a + ct * 128 + 16); }
      }
    }
  } else if (hw == 2) {
    const int rb = wg >> 2, cb = wg & 3;
    v4u w[7];
#pragma unroll
    for (int r = 0; r < 7; ++r) w[r] = __builtin_nontemporal_load((const GAS v4u*)(w2 + (size_t)(rb * 28 + g * 7 + r) * DM + cb * 128 + n * 8));
    const int t = lane < 56 ? lane : 0, tb = t / 28, tr = t % 28;
    const unsigned a = W_HG + (tb * 4 * FF + rb * 28 + tr) * 8;
    v2u pv[8];
    __builtin_amdgcn_s_sleep(127);
    for (unsigned spin = 0; spin < SPIN_CAP; ++spin) {
      __builtin_amdgcn_s_sleep(64);
      MEMFENCE();
      bool ok = true;
#pragma unroll
      for (int i = 0; i < 8; ++i) pv[i] = BLOAD8(a + ((i >> 2) * 8 + (i & 3)) * FF * 8);
#pragma unroll
      for (int i = 0; i < 8; ++i) ok &= gok1(pv[i], T);
      if (__all(ok)) break;
    }
    const float gt = rbf((gval(pv[0].x) + gval(pv[1].x)) + (gval(pv[2].x) + gval(pv[3].x))), up = rbf((gval(pv[4].x) + gval(pv[5].x)) + (gval(pv[6].x) + gval(pv[7].x)));
    const float hval = rbf(rbf(silu_f(gt)) * up);
    if (lane < 56) *LDSP(float, ldsbase + L_H + (tb * 32 + (tr / 7) * 8 + tr % 7) * 4) = hval;
    LGKM0();
#pragma unroll
    for (int bb = 0; bb < 2; ++bb) {
      const v4f h0 = *LDSP(const v4f, ldsbase + L_H + (bb * 32 + g * 8) * 4), h1 = *LDSP(const v4f, ldsbase + L_H + (bb * 32 + g * 8 + 4) * 4);
      const float hh[7] = {h0[0], h0[1], h0[2], h0[3], h1[0], h1[1], h1[2]};
      float z[8];
#pragma unroll
      for (int c = 0; c < 8; ++c) z[c] = 0.f;
#pragma unroll
      for (int r = 0; r < 7; ++r)
#pragma unroll
        for (int j = 0; j < 4; ++j) { z[2 * j] = fmaf(hh[r], lo16(w[r][j]), z[2 * j]); z[2 * j + 1] = fmaf(hh[r], hi16(w[r][j]), z[2 * j + 1]); }
      float t4[4];
#pragma unroll
      for (int j = 0; j < 4; ++j) t4[j] = swap32_add(z[j], z[j + 4]);
      BSTORE16(gran2(swap16_add(t4[0], t4[2]), swap16_add(t4[1], t4[3]), T), W_INZ + ((bb * 128 + 32 * cb + 2 * n + (g >> 1)) * 64 + rb) * 32 + (g & 1) * 16);
    }
  } else if (hw == 3) {
    if (wg == 0) {
      for (unsigned spin = 0; spin < SPIN_CAP; ++spin) {
        __builtin_amdgcn_s_sleep(32);
        MEMFENCE();
        bool ok = true;
#pragma unroll
        for (int i = 0; i < 4; ++i) ok &= gok1(BLOAD8(W_QG + (i * 64 + lane) * 128), T);
        if (__all(ok)) break;
      }
      if (lane == 0) {
        const v2u e = BLOAD8(W_EPOCH);
        const u64 tv = (((u64)e.y << 32) | e.x) + 1;
        __builtin_amdgcn_raw_buffer_store_b64(v2u{(unsigned)tv, (unsigned)(tv >> 32)}, rs, W_EPOCH, 0, 16);
      }
    }
    const unsigned xv = __builtin_nontemporal_load((const GAS unsigned*)(x + wg * 4 + (lane & 1) * 2));
    xr0 = lo16(xv); xr1 = hi16(xv);
    const unsigned a = W_INZ + wg * 2048 + lane * 16;
    v4u v[2];
    __builtin_amdgcn_s_sleep(127); __builtin_amdgcn_s_sleep(127); __builtin_amdgcn_s_sleep(127);
    for (unsigned spin = 0; spin < SPIN_CAP; ++spin) {
      __builtin_amdgcn_s_sleep(64);
      MEMFENCE();
      v[0] = BLOAD16(a); v[1] = BLOAD16(a + 1024);
      if (__all(gok2(v[0], T) & gok2(v[1], T))) break;
    }
    const float z0 = parity_sum(gval(v[0].x) + gval(v[1].x)), z1 = parity_sum(gval(v[0].z) + gval(v[1].z));
    gate0 = rbf(silu_f(rbf(z0))); gate1 = rbf(silu_f(rbf(z1)));
  }
  if (hw != 0) STAMP(1);
  if (WO_WHEN == 2) LOAD_WO();
  if (hw != 0) STAMP(2);
  BARRIER();
  STAMP(5);
  {
    unsigned op[4];
    if (hw == 0) {
      const unsigned a = W_IN1 + (wg * 16 + n) * 320;
      v4u v[4], ml;
      bool ok = false;
      for (unsigned spin = 0; spin < SPIN_CAP; ++spin) {
        MEMFENCE();
        if (!ok) {
#pragma unroll
          for (int i = 0; i < 4; ++i) v[i] = BLOAD16(a + g * 64 + i * 16);
          ml = BLOAD16(a + 256);
          ok = gok2(v[0], T) & gok2(v[1], T) & gok2(v[2], T) & gok2(v[3], T) & gok2(ml, T);
        }
        if (__all(ok)) break;
        if (POLL_SLEEP) __builtin_amdgcn_s_sleep(POLL_SLEEP);
      }
      STAMP(6);
      const float mp = gval(ml.x), lp = gval(ml.z);
      const float M = row_max(mp);
      const float wt = __builtin_amdgcn_exp2f((mp - M) * LOG2E);
      const float invL = __builtin_amdgcn_rcpf(row_sum(wt * lp));
#pragma unroll
      for (int i = 0; i < 4; ++i) op[i] = pk2(row_sum(wt * gval(v[i].x)) * invL, row_sum(wt * gval(v[i].z)) * invL);
      if (n == 0) *LDSP(v4u, ldsbase + L_OP + g * 16) = v4u{op[0], op[1], op[2], op[3]};
      LGKM0();
      if (lane == 0) *LDSP(volatile unsigned, ldsbase + L_OP + 64) = 1u;
    } else {
      for (unsigned spin = 0; spin < (SPIN_CAP << 8); ++spin) {
        if (*LDSP(volatile unsigned, ldsbase + L_OP + 64)) break;
        __builtin_amdgcn_s_sleep(1);
      }
      MEMFENCE();
      const v4u o4 = *LDSP(const v4u, ldsbase + L_OP + g * 16);
      op[0] = o4.x; op[1] = o4.y; op[2] = o4.z; op[3] = o4.w;
      STAMP(6);
    }
    float y[8];
#pragma unroll
    for (int c = 0; c < 8; ++c) { float acc = 0.f;
#pragma unroll
      for (int k = 0; k < 4; ++k) acc = dot2(wp[k][c], op[k], acc);
      y[c] = acc; }
    float t4[4];
#pragma unroll
    for (int j = 0; j < 4; ++j) t4[j] = swap32_add(y[j], y[j + 4]);
    const unsigned a2 = W_INY + ((b * 128 + 32 * hw + 2 * n + (g >> 1)) * 128 + (wg & 127)) * 32 + (g & 1) * 16;
    BSTORE16(gran2(swap16_add(t4[0], t4[2]), swap16_add(t4[1], t4[3]), T), a2);
  }
  if (hw == 3) {
    const unsigned a = W_INY + wg * 4096 + lane * 16;
    v4u v[4];
    bool ok[4] = {false, false, false, false};
    for (unsigned spin = 0; spin < SPIN_CAP; ++spin) {
      MEMFENCE();
#pragma unroll
      for (int i = 0; i < 4; ++i) if (!ok[i]) v[i] = BLOAD16(a + i * 1024);
#pragma unroll
      for (int i = 0; i < 4; ++i) ok[i] = gok2(v[i], T);
      if (__all(ok[0] & ok[1] & ok[2] & ok[3])) break;
      if (POLL_SLEEP) __builtin_amdgcn_s_sleep(POLL_SLEEP);
    }
    const float s0 = parity_sum((gval(v[0].x) + gval(v[1].x)) + (gval(v[2].x) + gval(v[3].x)));
    const float s1 = parity_sum((gval(v[0].z) + gval(v[1].z)) + (gval(v[2].z) + gval(v[3].z)));
    if (lane < 2) *(GAS unsigned*)(out + wg * 4 + lane * 2) = pk2(xr0 + rbf(rbf(s0) * gate0), xr1 + rbf(rbf(s1) * gate1));
  }
  STAMP(7); STAMP_FLUSH();
}

extern "C" void kernel_launch(void* const* d_in, const int* in_sizes, int n_in, void* d_out, int out_size, void* d_ws, size_t ws_size, hipStream_t stream) {
  (void)hipFuncSetAttribute((const void*)k_fused, hipFuncAttributeMaxDynamicSharedMemorySize, LDS_BYTES);
  k_fused<<<NWG, 512, LDS_BYTES, stream>>>((const u16*)d_in[0], (const u16*)d_in[1], (const u16*)d_in[2], (const u16*)d_in[3], (const u16*)d_in[4],
                                           (const u16*)d_in[6], (const u16*)d_in[7], (const u16*)d_in[8], (u16*)d_out, (unsigned char*)d_ws);
}
```

```cpp
#include <hip/hip_runtime.h>
#include <hip/hip_bf16.h>
#include <stdint.h>

#define GAS __attribute__((address_space(1)))
#define LAS __attribute__((address_space(3)))
typedef unsigned v4u __attribute__((ext_vector_type(4)));
typedef unsigned v2u __attribute__((ext_vector_type(2)));
typedef float v4f __attribute__((ext_vector_type(4)));
typedef float v2f __attribute__((ext_vector_type(2)));
typedef __bf16 v8bf __attribute__((ext_vector_type(8)));
typedef __bf16 v2bf __attribute__((ext_vector_type(2)));
typedef short v4s __attribute__((ext_vector_type(4)));
typedef short v8s __attribute__((ext_vector_type(8)));
typedef unsigned short u16;
typedef unsigned long long u64;

constexpr int DM = 512, SK = 32768, FF = 1792, NWG = 256;
constexpr float SCALE = 0.088388347648318440f, LOG2E = 1.4426950408889634f;
#define N0 31
#define N1 33
#define N2 31
#define N3 33
static_assert(N0 + N1 + N2 + N3 == SK / 16 / 16, "tiles per wave by side job: 4 waves x 4 workgroups x (N0 + .. + N3) = 2048 tiles of 16 keys per kv group");
#define H1ALL 0
#define DT 0
#define H1MODE 1
#define H2MODE 0
#define H1SL 3
#define H2SL 3
#define H1EAGER 2
#define T_Q 250
#define T_DOWN 1000
#define T_Z 1800
#define RB 120

constexpr int L_RING = 0, L_STAGE = 4 * 4 * 8192, L_XN = L_STAGE + 16384, L_QL = L_XN + 2048, L_MISC = L_QL + 1024, L_H = L_MISC + 256, L_OP = L_H + 1024, L_QP = L_OP + 256,
              LDS_BYTES = L_QP + 2048;
constexpr unsigned W_EPOCH = 0, W_ALIVE = 4096, W_QG = W_ALIVE + 2048, W_HG = W_QG + 2 * 4096 * 8, W_ML = W_HG + 4 * FF * 8, W_IN1 = W_ML + 16 * 1024,
                   W_INY = W_IN1 + NWG * 4096, W_INZ = W_INY + 64 * 16384, W_END = W_INZ + 64 * 2048;
constexpr unsigned SPIN_CAP = 1u << 16;

__device__ __forceinline__ float rbf(float f) { __bf16 b = (__bf16)f; return __uint_as_float((unsigned)__builtin_bit_cast(u16, b) << 16); }
__device__ __forceinline__ unsigned pk2(float lo, float hi) { v2bf p = {(__bf16)lo, (__bf16)hi}; return __builtin_bit_cast(unsigned, p); }
__device__ __forceinline__ float lo16(unsigned v) { return __uint_as_float(v << 16); }
__device__ __forceinline__ float hi16(unsigned v) { return __uint_as_float(v & 0xffff0000u); }
template <int CTRL> __device__ __forceinline__ float dpp(float x) {
  return __builtin_bit_cast(float, __builtin_amdgcn_mov_dpp(__builtin_bit_cast(int, x), CTRL, 0xf, 0xf, true));
}
constexpr int XOR1 = 0xB1, XOR2 = 0x4E, HMIR = 0x141, MIR = 0x140, ROR8 = 0x128;
__device__ __forceinline__ float swap32_add(float a, float b) { auto s = __builtin_amdgcn_permlane32_swap(__float_as_uint(a), __float_as_uint(b), false, false); return __uint_as_float(s[0]) + __uint_as_float(s[1]); }
__device__ __forceinline__ float swap16_add(float a, float b) { auto s = __builtin_amdgcn_permlane16_swap(__float_as_uint(a), __float_as_uint(b), false, false); return __uint_as_float(s[0]) + __uint_as_float(s[1]); }
__device__ __forceinline__ float xrow16_max(float x) {
  auto s = __builtin_amdgcn_permlane16_swap(__float_as_uint(x), __float_as_uint(x), false, false);
  x = fmaxf(__uint_as_float(s[0]), __uint_as_float(s[1]));
  auto t = __builtin_amdgcn_permlane32_swap(__float_as_uint(x), __float_as_uint(x), false, false);
  return fmaxf(__uint_as_float(t[0]), __uint_as_float(t[1]));
}
__device__ __forceinline__ float xrow16_sum(float x) { x = swap16_add(x, x); return swap32_add(x, x); }
__device__ __forceinline__ float row_sum(float x) { x += dpp<XOR1>(x); x += dpp<XOR2>(x); x += dpp<HMIR>(x); x += dpp<MIR>(x); return x; }
__device__ __forceinline__ float row_max(float x) { x = fmaxf(x, dpp<XOR1>(x)); x = fmaxf(x, dpp<XOR2>(x)); x = fmaxf(x, dpp<HMIR>(x)); x = fmaxf(x, dpp<MIR>(x)); return x; }
__device__ __forceinline__ float wave_sum(float x) { return xrow16_sum(row_sum(x)); }
__device__ __forceinline__ float silu_f(float x) { return x * __builtin_amdgcn_rcpf(1.f + __builtin_amdgcn_exp2f(-x * LOG2E)); }
__device__ __forceinline__ float dot2(unsigned a, unsigned b, float c) { return __builtin_amdgcn_fdot2_f32_bf16(__builtin_bit_cast(v2bf, a), __builtin_bit_cast(v2bf, b), c, false); }

#define STAMP(i) do {} while (0)
#define STAMP_DECL do {} while (0)
#define STAMP_FLUSH() do {} while (0)

struct Tag { unsigned lo, hi; };
__device__ __forceinline__ Tag make_tag(v2u e) { const u64 t = (((u64)e.y << 32) | e.x) + 1; return Tag{(unsigned)(t & 0xff) << 24, (unsigned)(t >> 8)}; }
__device__ __forceinline__ v4u gran2(float a, float b, Tag t) { return v4u{(__float_as_uint(a) >> 8) | t.lo, t.hi, (__float_as_uint(b) >> 8) | t.lo, t.hi}; }
__device__ __forceinline__ bool gok2(v4u v, Tag t) { return (v.y == t.hi) & (v.w == t.hi) & (((v.x ^ t.lo) | (v.z ^ t.lo)) < 0x1000000u); }
__device__ __forceinline__ float gval(unsigned lo) { return __uint_as_float(lo << 8); }
#define BLOAD16(off) __builtin_amdgcn_raw_buffer_load_b128(rs, (off), 0, 16)
#define BSTORE16(v, off) __builtin_amdgcn_raw_buffer_store_b128((v), rs, (off), 0, 16)
#define MEMFENCE() asm volatile("" ::: "memory")
#define LGKM0() asm volatile("s_waitcnt lgkmcnt(0)" ::: "memory")
#define VMCNT(n) asm volatile("s_waitcnt vmcnt(" #n ")" ::: "memory")
#define BARRIER() do { LGKM0(); __builtin_amdgcn_s_barrier(); MEMFENCE(); } while (0)
#define LDSP(T, a) ((LAS T*)(uintptr_t)(a))
#define LDS_SPIN(cond) do { for (unsigned spin_ = 0; spin_ < (SPIN_CAP << 8); ++spin_) { if (cond) break; __builtin_amdgcn_s_sleep(1); } MEMFENCE(); } while (0)

__device__ __forceinline__ void dma1(const void* gsrc, unsigned lds_dst) {
  unsigned keep;
  asm volatile("s_mov_b32 %0, m0\n\ts_mov_b32 m0, %2\n\ts_nop 0\n\tglobal_load_lds_dwordx4 %1, off nt\n\ts_mov_b32 m0, %0" : "=&s"(keep) : "v"(gsrc), "s"(lds_dst) : "memory");
}
__device__ __forceinline__ void dma4(unsigned oe, unsigned oo, const char* gb, unsigned lds_dst) {
  unsigned keep;
  asm volatile("s_mov_b32 %0, m0\n\ts_mov_b32 m0, %4\n\ts_nop 0\n\t"
               "global_load_lds_dwordx4 %1, %3 nt\n\tglobal_load_lds_dwordx4 %2, %3 offset:1024 nt\n\t"
               "global_load_lds_dwordx4 %1, %3 offset:2048 nt\n\tglobal_load_lds_dwordx4 %2, %3 offset:3072 nt\n\ts_mov_b32 m0, %0"
               : "=&s"(keep) : "v"(oe), "v"(oo), "s"(gb), "s"(lds_dst) : "memory");
}
template <int R> __device__ __forceinline__ void park4(unsigned oe, unsigned oo, const char* gb) {
  asm volatile("global_load_dwordx4 v[%c3+0:%c3+3], %0, %2 nt\n\tglobal_load_dwordx4 v[%c3+4:%c3+7], %1, %2 offset:1024 nt\n\t"
               "global_load_dwordx4 v[%c3+8:%c3+11], %0, %2 offset:2048 nt\n\tglobal_load_dwordx4 v[%c3+12:%c3+15], %1, %2 offset:3072 nt"
               :: "v"(oe), "v"(oo), "s"(gb), "i"(R) : "memory");
}
template <int R> __device__ __forceinline__ void unpark(unsigned addr) {
  asm volatile("ds_write_b128 %0, v[%c1+0:%c1+3]\n\tds_write_b128 %0, v[%c1+4:%c1+7] offset:1024\n\tds_write_b128 %0, v[%c1+8:%c1+11] offset:2048\n\t"
               "ds_write_b128 %0, v[%c1+12:%c1+15] offset:3072\n\tds_write_b128 %0, v[%c1+16:%c1+19] offset:4096\n\tds_write_b128 %0, v[%c1+20:%c1+23] offset:5120\n\t"
               "ds_write_b128 %0, v[%c1+24:%c1+27] offset:6144\n\tds_write_b128 %0, v[%c1+28:%c1+31] offset:7168\n\ts_waitcnt lgkmcnt(0)"
               :: "v"(addr), "i"(R) : "memory");
}
__device__ __forceinline__ v4f mfma_tr(unsigned a_lo, unsigned a_hi, v8s bb, v4f acc) {
  const v4s a0 = __builtin_amdgcn_ds_read_tr16_b64_v4i16(LDSP(v4s, a_lo));
  const v4s a1 = __builtin_amdgcn_ds_read_tr16_b64_v4i16(LDSP(v4s, a_hi));
  const v8s a = __builtin_shufflevector(a0, a1, 0, 1, 2, 3, 4, 5, 6, 7);
  return __builtin_amdgcn_mfma_f32_16x16x32_bf16(__builtin_bit_cast(v8bf, a), __builtin_bit_cast(v8bf, bb), acc, 0, 0, 0);
}

__global__ void __launch_bounds__(512, 2) k_fused(const u16* __restrict__ x, const u16* __restrict__ Kc, const u16* __restrict__ Vc, const u16* __restrict__ wq,
                                                  const u16* __restrict__ wo, const u16* __restrict__ w1, const u16* __restrict__ w3, const u16* __restrict__ w2,
                                                  u16* __restrict__ out, unsigned char* __restrict__ ws) {
  extern __shared__ __attribute__((aligned(16))) unsigned char lds[];
  const int wave = __builtin_amdgcn_readfirstlane(threadIdx.x >> 6);
  const int wg = blockIdx.x, grp = wg >> 4, sp = wg & 15, b = grp >> 3, kvh = grp & 7;
  const int job = (sp >> 2) ^ (grp & 1), jidx = grp * 4 + (sp & 3);
  const bool pj = job == 0 || (job == 2 && jidx < 56);
  const unsigned ldsbase = (unsigned)(uintptr_t)(LAS unsigned char*)lds;
  const __amdgpu_buffer_rsrc_t rs = __builtin_amdgcn_make_buffer_rsrc(ws, 0, W_END, 0x00020000);
  STAMP_DECL; STAMP(0);

  if (wave < 4) {
    asm volatile("; SPATH_BEGIN" ::: "v247");
    int lane;
    asm volatile("v_mbcnt_lo_u32_b32 %0, -1, 0\n\tv_mbcnt_hi_u32_b32 %0, -1, %0" : "=v"(lane));
    const int n = lane & 15, g = lane >> 4;
    const unsigned ring = ldsbase + L_RING + wave * 32768;
    const unsigned koe = g * 256 + ((n & 8) | ((n & 7) ^ g)) * 16, koo = g * 256 + ((n & 8) | ((n & 7) ^ (4 + g))) * 16;
    const unsigned voe = g * 256 + (n ^ (g << 1)) * 16, voo = g * 256 + (n ^ ((4 + g) << 1)) * 16;
    const int gx = grp & 1, qd = sp >> 2;
    const int c0 = gx ? N1 : N0, c1 = gx ? N0 : N1, c2 = gx ? N3 : N2;
    const int nt = job == 0 ? N0 : job == 1 ? N1 : job == 2 ? N2 : N3;
    const int t0s = 4 * ((qd > 0 ? c0 : 0) + (qd > 1 ? c1 : 0) + (qd > 2 ? c2 : 0)) + (sp & 3) * nt;
    const size_t gbase = (size_t)grp * SK * 256 + (size_t)wave * 4096 + (size_t)t0s * 16384;
    const char* kptr = (const char*)Kc + gbase;
    const char* vptr = (const char*)Vc + gbase;
#define ISSUE_K(t, slot) dma4(koe, koo, kptr + (size_t)(t) * 16384, ring + (slot) * 8192)
#define ISSUE_V(t, slot) dma4(voe, voo, vptr + (size_t)(t) * 16384, ring + (slot) * 8192 + 4096)
#define PARK(r) do { VMCNT(4); park4<RB + 32 * (r)>(koe, koo, kptr + (size_t)(4 + (r)) * 16384); VMCNT(4); park4<RB + 32 * (r) + 16>(voe, voo, vptr + (size_t)(4 + (r)) * 16384); } while (0)
    if (pj) { __builtin_amdgcn_s_barrier(); LDS_SPIN(*LDSP(volatile unsigned, ldsbase + L_MISC + 32 + (wave ? wave : 1) * 4)); }
    ISSUE_K(0, 0); ISSUE_V(0, 0);
    if (!pj) __builtin_amdgcn_s_barrier();
    VMCNT(4); ISSUE_K(1, 1); VMCNT(4); ISSUE_V(1, 1);
    STAMP(1);
    VMCNT(4); ISSUE_K(2, 2); VMCNT(4); ISSUE_V(2, 2);
    VMCNT(4); ISSUE_K(3, 3); VMCNT(4); ISSUE_V(3, 3);
    PARK(0); PARK(1); PARK(2); PARK(3);
    unsigned ka[4], va[8];
#pragma unroll
    for (int t = 0; t < 4; ++t) ka[t] = ring + n * 256 + ((8 * (g & 1)) | ((4 * (g >> 1) + t) ^ (n & 7))) * 16;
    { const unsigned row = lane >> 2;
#pragma unroll
      for (int dt = 0; dt < 8; ++dt) va[dt] = ring + 4096 + row * 256 + ((dt ^ (row & 7)) << 5) + (lane & 3) * 8; }
    const unsigned wa2 = ring + 2 * 8192 + lane * 16, wa3 = wa2 + 8192;
    STAMP(2);
    LDS_SPIN(*LDSP(volatile unsigned, ldsbase + L_MISC + 16));
    STAMP(3);
    v8bf qf[4];
#pragma unroll
    for (int t = 0; t < 4; ++t) qf[t] = __builtin_bit_cast(v8bf, *LDSP(const v4u, ldsbase + L_QL + (n & 3) * 256 + (8 * (g & 1) + 4 * (g >> 1) + t) * 16));
    float m = -INFINITY, l = 0.f;
    v4f O[8];
#pragma unroll
    for (int dt = 0; dt < 8; ++dt) O[dt] = v4f{0, 0, 0, 0};
    for (int j = 0; j < nt; ++j) {
      int slot = j & 3;
      if (j >= 8) {
        if (DT && j > nt - DT) {
          if (j + 2 < nt) VMCNT(16); else if (j + 1 < nt) VMCNT(8); else VMCNT(0);
          if (j + 3 < nt) { ISSUE_K(j + 3, (j + 3) & 3); ISSUE_V(j + 3, (j + 3) & 3); }
        } else {
          if (j + 1 < nt) VMCNT(8); else VMCNT(0);
          if (j + 2 < nt) { ISSUE_K(j + 2, (j + 2) & 3); ISSUE_V(j + 2, (j + 2) & 3); }
          if (DT && j == nt - DT) { ISSUE_K(j + 3, (j + 3) & 3); ISSUE_V(j + 3, (j + 3) & 3); }
        }
      } else if (j >= 2) {
        slot = 2 + (j & 1);
        if (j == 2) { VMCNT(8); ISSUE_K(8, 0); ISSUE_V(8, 0); }
        else if (j == 4) unpark<RB>(wa2);
        else if (j == 5) unpark<RB + 32>(wa3);
        else if (j == 6) { VMCNT(8); ISSUE_K(9, 1); ISSUE_V(9, 1); unpark<RB + 64>(wa2); }
        else if (j == 7) unpark<RB + 96>(wa3);
      }
      const unsigned so = slot * 8192;
      v4u kf[4]; v4s vf[8];
#pragma unroll
      for (int t = 0; t < 4; ++t) kf[t] = *LDSP(const v4u, ka[t] + so);
#pragma unroll
      for (int dt = 0; dt < 8; ++dt) vf[dt] = __builtin_amdgcn_ds_read_tr16_b64_v4i16(LDSP(v4s, va[dt] + so));
      v4f S = {0, 0, 0, 0};
#pragma unroll
      for (int t = 0; t < 4; ++t) S = __builtin_amdgcn_mfma_f32_16x16x32_bf16(__builtin_bit_cast(v8bf, kf[t]), qf[t], S, 0, 0, 0);
      const unsigned a01 = pk2(S[0], S[1]), a23 = pk2(S[2], S[3]);
      const unsigned c01 = pk2(lo16(a01) * SCALE, hi16(a01) * SCALE), c23 = pk2(lo16(a23) * SCALE, hi16(a23) * SCALE);
      const float s0 = lo16(c01), s1 = hi16(c01), s2 = lo16(c23), s3 = hi16(c23);
      const float tm = xrow16_max(fmaxf(fmaxf(s0, s1), fmaxf(s2, s3)));
      if (__any(tm > m)) {
        const float mn = fmaxf(m, tm);
        const float alpha = __builtin_amdgcn_exp2f((m - mn) * LOG2E);
        l *= alpha;
#pragma unroll
        for (int dt = 0; dt < 8; ++dt) O[dt] *= alpha;
        m = mn;
      }
      const float p0 = __builtin_amdgcn_exp2f((s0 - m) * LOG2E), p1 = __builtin_amdgcn_exp2f((s1 - m) * LOG2E);
      const float p2 = __builtin_amdgcn_exp2f((s2 - m) * LOG2E), p3 = __builtin_amdgcn_exp2f((s3 - m) * LOG2E);
      l += (p0 + p1) + (p2 + p3);
      const v2u pp = {pk2(p0, p1), pk2(p2, p3)};
      const v4s pf = __builtin_bit_cast(v4s, pp);
#pragma unroll
      for (int dt = 0; dt < 8; ++dt) O[dt] = __builtin_amdgcn_mfma_f32_16x16x16bf16_1k(vf[dt], pf, O[dt], 0, 0, 0);
      LGKM0();
    }
    STAMP(4);
    l = xrow16_sum(l);
    if (n < 4) {
      const unsigned r = ring + n * 528;
#pragma unroll
      for (int dt = 0; dt < 8; ++dt) *LDSP(v4f, r + 16 + (16 * dt + 4 * g) * 4) = O[dt];
      if (g == 0) *LDSP(v2f, r) = v2f{m, l};
    }
    BARRIER();
    STAMP(5);
    {
      const v2u tg = *LDSP(const v2u, ldsbase + L_MISC);
      const Tag T{tg.x, tg.y};
      v2f ml[4], ov[4];
#pragma unroll
      for (int w = 0; w < 4; ++w) { const unsigned r = ldsbase + L_RING + w * 32768 + wave * 528; ml[w] = *LDSP(const v2f, r); ov[w] = *LDSP(const v2f, r + 16 + lane * 8); }
      const float M = fmaxf(fmaxf(ml[0].x, ml[1].x), fmaxf(ml[2].x, ml[3].x));
      float L = 0, o0 = 0, o1 = 0;
#pragma unroll
      for (int w = 0; w < 4; ++w) { const float wt = __builtin_amdgcn_exp2f((ml[w].x - M) * LOG2E); L = fmaf(wt, ml[w].y, L); o0 = fmaf(wt, ov[w].x, o0); o1 = fmaf(wt, ov[w].y, o1); }
      BSTORE16(gran2(o0, o1, T), W_IN1 + ((grp * 16 + 4 * wave + g) * 16 + sp) * 256 + n * 16);
      if (lane == 0) BSTORE16(gran2(M, L, T), W_ML + (grp * 16 + sp) * 64 + wave * 16);
    }
    STAMP(6); STAMP_FLUSH();
    asm volatile("; SPATH_END");
    return;
  }

  const int lane = threadIdx.x & 63, n = lane & 15, g = lane >> 4, hw = wave - 4;
  const u64 tk0 = __builtin_amdgcn_s_memrealtime();
#define WAIT_UNTIL(ticks) do { while (__builtin_amdgcn_s_memrealtime() - tk0 < (u64)(ticks)) __builtin_amdgcn_s_sleep(16); } while (0)
  Tag T;
  v4u w[28];
  {
    v2u ep;
    const unsigned char* e = ws + W_EPOCH;
    if (pj) {
      const int mat = jidx >= 28, cbk = jidx - 28 * mat;
      const char* wsrc = job == 0 ? (const char*)wq + jidx * 128 : (const char*)(mat ? w3 : w1) + cbk * 128;
      const unsigned ldw = job == 0 ? 8192u : (unsigned)(FF * 2);
      const unsigned obase = job == 0 ? W_QG + 64 * jidx * 8 : W_HG + (mat * 2 * FF + 64 * cbk) * 8, ostride = job == 0 ? 4096u * 8 : (unsigned)(FF * 8);
      const unsigned stg = hw == 0 ? ldsbase + L_STAGE : ldsbase + L_RING + hw * 32768 + 16384;
      v4u x0, x1;
      { const u16* xp = x + lane * 8;
        asm volatile("global_load_dwordx4 %0, %3, off nt\n\tglobal_load_dwordx4 %1, %3, off offset:1024 nt\n\tglobal_load_dwordx2 %2, %4, off nt" : "=&v"(x0), "=&v"(x1), "=&v"(ep) : "v"(xp), "v"(e) : "memory"); }
      { const char* src = wsrc + (size_t)(128 * hw + (lane >> 3)) * ldw + (lane & 7) * 16;
#pragma unroll
        for (int i = 0; i < 16; ++i) dma1(src + (size_t)i * (8 * ldw), stg + i * 1024); }
      if (hw == 0 && lane < 3) *LDSP(v4u, ldsbase + L_MISC + 16 + lane * 16) = v4u{0, 0, 0, 0};
      BARRIER();
      asm volatile("s_waitcnt vmcnt(16)" : "+v"(x0), "+v"(x1), "+v"(ep) :: "memory");
      STAMP(1);
      {
        float v[2][8];
#pragma unroll
        for (int j = 0; j < 4; ++j) { v[0][2 * j] = lo16(x0[j]); v[0][2 * j + 1] = hi16(x0[j]); v[1][2 * j] = lo16(x1[j]); v[1][2 * j + 1] = hi16(x1[j]); }
#pragma unroll
        for (int bb = 0; bb < 2; ++bb) {
          const float s_ = ((v[bb][0] + v[bb][1]) + (v[bb][2] + v[bb][3])) + ((v[bb][4] + v[bb][5]) + (v[bb][6] + v[bb][7]));
          const float mean = wave_sum(s_) * (1.f / DM);
          float q_ = 0;
#pragma unroll
          for (int j = 0; j < 8; ++j) { const float d = v[bb][j] - mean; q_ = fmaf(d, d, q_); }
          const float var = wave_sum(q_) * (1.f / DM);
          const float mb = rbf(mean);
          const float inv = __builtin_amdgcn_rcpf(rbf(__builtin_amdgcn_sqrtf(rbf(rbf(var) + 1e-5f))));
          v4u o;
#pragma unroll
          for (int j = 0; j < 4; ++j) o[j] = pk2(rbf(v[bb][2 * j] - mb) * inv, rbf(v[bb][2 * j + 1] - mb) * inv);
          const unsigned xa = ldsbase + L_XN + bb * 1024 + (lane >> 2) * 64 + (lane & 1) * 32 + ((lane >> 1) & 1) * 8;
          if (g == hw) { *LDSP(v2u, xa) = v2u{o[0], o[1]}; *LDSP(v2u, xa + 16) = v2u{o[2], o[3]}; }
        }
      }
      asm volatile("s_waitcnt vmcnt(0) lgkmcnt(0)" ::: "memory");
      STAMP(2);
      v4f acc[4];
#pragma unroll
      for (int ct = 0; ct < 4; ++ct) acc[ct] = v4f{0, 0, 0, 0};
      { const unsigned aaddr = stg + (lane >> 2) * 128 + (lane & 3) * 8, baddr = ldsbase + L_XN + (lane & 1) * 1024 + hw * 256 + g * 16;
#pragma unroll
        for (int s_ = 0; s_ < 4; ++s_) { const v8s bb = *LDSP(const v8s, baddr + s_ * 64);
#pragma unroll
          for (int ct = 0; ct < 4; ++ct) acc[ct] = mfma_tr(aaddr + s_ * 4096 + ct * 32, aaddr + s_ * 4096 + 2048 + ct * 32, bb, acc[ct]); } }
      if (n < 2) {
#pragma unroll
        for (int ct = 0; ct < 4; ++ct) *LDSP(v4f, ldsbase + L_QP + hw * 512 + (ct * 8 + g * 2 + n) * 16) = acc[ct];
      }
      LGKM0();
      if (hw != 0) {
        if (lane == 0) *LDSP(volatile unsigned, ldsbase + L_MISC + 32 + hw * 4) = 1u;
      } else {
        T = make_tag(ep);
        for (unsigned spin = 0; spin < (SPIN_CAP << 8); ++spin) { const v4u f = *LDSP(volatile v4u, ldsbase + L_MISC + 32); if (f.y & f.z & f.w) break; }
        MEMFENCE();
        if (n < 8) {
          const unsigned qa = ldsbase + L_QP + ((n >> 1) * 8 + g * 2 + (n & 1)) * 16;
          const v4f q = (*LDSP(const v4f, qa) + *LDSP(const v4f, qa + 512)) + (*LDSP(const v4f, qa + 1024) + *LDSP(const v4f, qa + 1536));
          const unsigned a = obase + (n & 1) * ostride + (16 * (n >> 1) + 4 * g) * 8;
          BSTORE16(gran2(rbf(q[0]), rbf(q[1]), T), a);
          BSTORE16(gran2(rbf(q[2]), rbf(q[3]), T), a + 16);
        }
        STAMP(3);
      }
    } else {
      if (hw == 0) {
        asm volatile("global_load_dwordx2 %0, %1, off nt" : "=&v"(ep) : "v"(e) : "memory");
        if (lane < 3) *LDSP(v4u, ldsbase + L_MISC + 16 + lane * 16) = v4u{0, 0, 0, 0};
      }
      BARRIER();
      if (hw == 2 && job == 1) {
#pragma unroll
        for (int r = 0; r < 28; ++r) w[r] = __builtin_nontemporal_load((const GAS v4u*)(w2 + (size_t)((jidx >> 2) * 112 + g * 28 + r) * DM + (jidx & 3) * 128 + n * 8));
      }
      if (hw == 0) {
        asm volatile("s_waitcnt vmcnt(0)" : "+v"(ep) :: "memory");
        T = make_tag(ep);
        WAIT_UNTIL(T_Q);
      }
    }
    if (hw == 0) {
      if (lane == 0) __builtin_amdgcn_raw_buffer_store_b64(v2u{T.lo, T.hi}, rs, W_ALIVE + wg * 8, 0, 16);
      const unsigned a = W_QG + (b * 4096 + kvh * 512) * 8 + lane * 16;
      v4u v[4];
      bool ok[4] = {false, false, false, false};
      for (unsigned spin = 0; spin < SPIN_CAP; ++spin) {
        MEMFENCE();
#pragma unroll
        for (int i = 0; i < 4; ++i) if (!ok[i]) v[i] = BLOAD16(a + i * 1024);
#pragma unroll
        for (int i = 0; i < 4; ++i) ok[i] = gok2(v[i], T);
        if (__all(ok[0] & ok[1] & ok[2] & ok[3])) break;
        __builtin_amdgcn_s_sleep(4);
      }
#pragma unroll
      for (int i = 0; i < 4; ++i) *LDSP(unsigned, ldsbase + L_QL + i * 256 + lane * 4) = ((v[i].x >> 8) & 0xffffu) | ((v[i].z << 8) & 0xffff0000u);
      if (lane == 0) *LDSP(v2u, ldsbase + L_MISC) = v2u{T.lo, T.hi};
      LGKM0();
      if (lane == 0) *LDSP(volatile unsigned, ldsbase + L_MISC + 16) = 1u;
      STAMP(4);
    } else {
      LDS_SPIN(*LDSP(volatile unsigned, ldsbase + L_MISC + 16));
      const v2u tg = *LDSP(const v2u, ldsbase + L_MISC); T = Tag{tg.x, tg.y};
    }
  }
  if (hw == 2 && job == 1) {
    const int rb = jidx >> 2, cb = jidx & 3;
    const unsigned a = W_HG + (rb * 112 + 2 * (lane < 56 ? lane : 0)) * 8;
    v4u pv[4];
    WAIT_UNTIL(T_DOWN);
    for (unsigned spin = 0; spin < SPIN_CAP; ++spin) {
      if (spin) __builtin_amdgcn_s_sleep(64);
      MEMFENCE();
#pragma unroll
      for (int i = 0; i < 4; ++i) pv[i] = BLOAD16(a + i * FF * 8);
      if (__all(gok2(pv[0], T) & gok2(pv[1], T) & gok2(pv[2], T) & gok2(pv[3], T))) break;
    }
    if (lane < 56) {
#pragma unroll
      for (int bb = 0; bb < 2; ++bb)
        *LDSP(v2f, ldsbase + L_H + bb * 512 + lane * 8) = v2f{rbf(rbf(silu_f(gval(pv[bb].x))) * gval(pv[2 + bb].x)), rbf(rbf(silu_f(gval(pv[bb].z))) * gval(pv[2 + bb].z))};
    }
    LGKM0();
    float z[2][8];
#pragma unroll
    for (int c = 0; c < 8; ++c) z[0][c] = z[1][c] = 0.f;
#pragma unroll
    for (int ch = 0; ch < 7; ++ch) {
      const v4f h0 = *LDSP(const v4f, ldsbase + L_H + (g * 28 + ch * 4) * 4), h1 = *LDSP(const v4f, ldsbase + L_H + 512 + (g * 28 + ch * 4) * 4);
#pragma unroll
      for (int r = 0; r < 4; ++r)
#pragma unroll
        for (int j = 0; j < 4; ++j) { const float wl = lo16(w[ch * 4 + r][j]), wh = hi16(w[ch * 4 + r][j]);
          z[0][2 * j] = fmaf(h0[r], wl, z[0][2 * j]); z[0][2 * j + 1] = fmaf(h0[r], wh, z[0][2 * j + 1]);
          z[1][2 * j] = fmaf(h1[r], wl, z[1][2 * j]); z[1][2 * j + 1] = fmaf(h1[r], wh, z[1][2 * j + 1]); }
    }
#pragma unroll
    for (int bb = 0; bb < 2; ++bb) {
      float t4[4];
#pragma unroll
      for (int j = 0; j < 4; ++j) t4[j] = swap32_add(z[bb][j], z[bb][j + 4]);
      BSTORE16(gran2(swap16_add(t4[0], t4[2]), swap16_add(t4[1], t4[3]), T), W_INZ + ((bb * 32 + 8 * cb + (n >> 1)) * 16 + rb) * 128 + ((n & 1) * 4 + g) * 16);
    }
  }
  if (hw == 3 && wg == 0) {
    WAIT_UNTIL(T_DOWN);
    for (unsigned spin = 0; spin < SPIN_CAP; ++spin) {
      if (spin) __builtin_amdgcn_s_sleep(64);
      MEMFENCE();
      const v4u a0 = BLOAD16(W_ALIVE + lane * 16), a1 = BLOAD16(W_ALIVE + 1024 + lane * 16);
      if (__all(gok2(a0, T) & gok2(a1, T))) break;
    }
    if (lane == 0) {
      const v2u e = __builtin_amdgcn_raw_buffer_load_b64(rs, W_EPOCH, 0, 16);
      const u64 tv = (((u64)e.y << 32) | e.x) + 1;
      __builtin_amdgcn_raw_buffer_store_b64(v2u{(unsigned)tv, (unsigned)(tv >> 32)}, rs, W_EPOCH, 0, 16);
    }
  }
  float xr0 = 0.f, xr1 = 0.f;
  if (job == 3) {
    const unsigned xv = __builtin_nontemporal_load((const GAS unsigned*)(x + jidx * 16 + (lane & 7) * 2));
    xr0 = lo16(xv); xr1 = hi16(xv);
    if (hw == 3) {
      const unsigned a = W_INZ + jidx * 2048 + lane * 16;
      v4u v[2];
      WAIT_UNTIL(T_Z);
      for (unsigned spin = 0; spin < SPIN_CAP; ++spin) {
        if (spin) __builtin_amdgcn_s_sleep(64);
        MEMFENCE();
        v[0] = BLOAD16(a); v[1] = BLOAD16(a + 1024);
        if (__all(gok2(v[0], T) & gok2(v[1], T))) break;
      }
      float z0 = gval(v[0].x) + gval(v[1].x), z1 = gval(v[0].z) + gval(v[1].z);
      z0 += dpp<ROR8>(z0); z1 += dpp<ROR8>(z1);
      z0 = xrow16_sum(z0); z1 = xrow16_sum(z1);
      if (lane < 8) *LDSP(v2f, ldsbase + L_H + lane * 8) = v2f{rbf(silu_f(rbf(z0))), rbf(silu_f(rbf(z1)))};
    }
  }
  if (hw != 0) STAMP(1);
  unsigned wp[4][8];
  {
    const int row0 = (kvh * 4 + (sp >> 2)) * 128 + (sp & 3) * 32 + 8 * g;
    v4u w_[8];
#pragma unroll
    for (int i = 0; i < 8; ++i) w_[i] = __builtin_nontemporal_load((const GAS v4u*)(wo + (size_t)(row0 + i) * DM + 128 * hw + 8 * n));
#pragma unroll
    for (int k = 0; k < 4; ++k)
#pragma unroll
      for (int j = 0; j < 4; ++j) { wp[k][2 * j] = __builtin_amdgcn_perm(w_[2 * k + 1][j], w_[2 * k][j], 0x05040100u); wp[k][2 * j + 1] = __builtin_amdgcn_perm(w_[2 * k + 1][j], w_[2 * k][j], 0x07060302u); }
#pragma unroll
    for (int k = 0; k < 4; ++k)
#pragma unroll
      for (int j = 0; j < 8; ++j) asm volatile("" : "+v"(wp[k][j]));
  }
  if (hw != 0) STAMP(2);
  BARRIER();
  STAMP(5);
  {
    unsigned op[4];
    if (H1ALL || hw == 0) {
      const unsigned a = W_IN1 + (wg * 16 + n) * 256 + g * 64, am = W_ML + (grp * 16 + n) * 64 + (sp >> 2) * 16;
      v4u v[4], ml;
      bool ok = false, mok = false, eager = !H1MODE;
      for (unsigned spin = 0; spin < SPIN_CAP; ++spin) {
        MEMFENCE();
        const bool ld = !ok & (mok | eager);
        if (!mok) ml = BLOAD16(am);
        if (ld) {
#pragma unroll
          for (int i = 0; i < 4; ++i) v[i] = BLOAD16(a + i * 16);
          ok = gok2(v[0], T) & gok2(v[1], T) & gok2(v[2], T) & gok2(v[3], T);
        }
        mok = gok2(ml, T);
        const u64 miss = __ballot(!mok);
        if (__all(ok & mok)) break;
        if (H1MODE) { eager = __builtin_popcountll(miss) <= 4 * H1EAGER; if (!eager) __builtin_amdgcn_s_sleep(H1SL); }
      }
      STAMP(6);
      const float mp = gval(ml.x), lp = gval(ml.z);
      const float M = row_max(mp);
      const float wt = __builtin_amdgcn_exp2f((mp - M) * LOG2E);
      const float invL = __builtin_amdgcn_rcpf(row_sum(wt * lp));
#pragma unroll
      for (int i = 0; i < 4; ++i) op[i] = pk2(row_sum(wt * gval(v[i].x)) * invL, row_sum(wt * gval(v[i].z)) * invL);
      if (!H1ALL) {
        if (n == 0) *LDSP(v4u, ldsbase + L_OP + g * 16) = v4u{op[0], op[1], op[2], op[3]};
        LGKM0();
        if (lane == 0) *LDSP(volatile unsigned, ldsbase + L_MISC + 20) = 1u;
      }
    } else {
      LDS_SPIN(*LDSP(volatile unsigned, ldsbase + L_MISC + 20));
      const v4u o4 = *LDSP(const v4u, ldsbase + L_OP + g * 16);
      op[0] = o4.x; op[1] = o4.y; op[2] = o4.z; op[3] = o4.w;
      STAMP(6);
    }
    float y[8];
#pragma unroll
    for (int c = 0; c < 8; ++c) { float acc = 0.f;
#pragma unroll
      for (int k = 0; k < 4; ++k) acc = dot2(wp[k][c], op[k], acc);
      y[c] = acc; }
    float t4[4];
#pragma unroll
    for (int j = 0; j < 4; ++j) t4[j] = swap32_add(y[j], y[j + 4]);
    BSTORE16(gran2(swap16_add(t4[0], t4[2]), swap16_add(t4[1], t4[3]), T), W_INY + ((b * 32 + 8 * hw + (n >> 1)) * 128 + (wg & 127)) * 128 + ((n & 1) * 4 + g) * 16);
  }
  if (job == 3) {
    const unsigned a = W_INY + jidx * 16384 + hw * 4096 + lane * 16;
    v4u v[4];
#pragma unroll
    for (int i = 0; i < 4; ++i) v[i] = v4u{~T.lo, ~T.hi, ~T.lo, ~T.hi};
    bool ok[4] = {false, false, false, false};
    bool ga = !H2MODE, gb = !H2MODE;
    for (unsigned spin = 0; spin < SPIN_CAP; ++spin) {
      MEMFENCE();
      if (!ok[0] & (ga | (lane < 8))) v[0] = BLOAD16(a);
      if (!ok[1] & ga) v[1] = BLOAD16(a + 1024);
      if (!ok[2] & (gb | (lane < 8))) v[2] = BLOAD16(a + 2048);
      if (!ok[3] & gb) v[3] = BLOAD16(a + 3072);
#pragma unroll
      for (int i = 0; i < 4; ++i) ok[i] = gok2(v[i], T);
      if (__all(ok[0] & ok[1] & ok[2] & ok[3])) break;
      if (H2MODE) {
        ga = (__ballot(ok[0]) & 1) != 0; gb = (__ballot(ok[2]) & 1) != 0;
        if (!(__all(ok[0] & ok[1]) | !ga) | !(__all(ok[2] & ok[3]) | !gb)) continue;
        if (H2SL) __builtin_amdgcn_s_sleep(H2SL);
      }
    }
    float s0 = (gval(v[0].x) + gval(v[1].x)) + (gval(v[2].x) + gval(v[3].x)), s1 = (gval(v[0].z) + gval(v[1].z)) + (gval(v[2].z) + gval(v[3].z));
    s0 += dpp<ROR8>(s0); s1 += dpp<ROR8>(s1);
    s0 = xrow16_sum(s0); s1 = xrow16_sum(s1);
    if (lane < 8) *LDSP(v2f, ldsbase + L_QP + hw * 64 + lane * 8) = v2f{s0, s1};
    LGKM0();
    unsigned old = 0;
    if (lane == 0) old = __hip_atomic_fetch_add(LDSP(unsigned, ldsbase + L_MISC + 24), 1u, __ATOMIC_RELAXED, __HIP_MEMORY_SCOPE_WORKGROUP);
    old = __builtin_amdgcn_readfirstlane(old);
    MEMFENCE();
    if (old == 3 && lane < 8) {
      const unsigned pa = ldsbase + L_QP + lane * 8;
      const v2f t = (*LDSP(const v2f, pa) + *LDSP(const v2f, pa + 64)) + (*LDSP(const v2f, pa + 128) + *LDSP(const v2f, pa + 192));
      const v2f gt = *LDSP(const v2f, ldsbase + L_H + lane * 8);
      *(GAS unsigned*)(out + jidx * 16 + lane * 2) = pk2(xr0 + rbf(rbf(t.x) * gt.x), xr1 + rbf(rbf(t.y) * gt.y));
    }
  }
  STAMP(7); STAMP_FLUSH();
}

extern "C" void kernel_launch(void* const* d_in, const int* in_sizes, int n_in, void* d_out, int out_size, void* d_ws, size_t ws_size, hipStream_t stream) {
  (void)hipFuncSetAttribute((const void*)k_fused, hipFuncAttributeMaxDynamicSharedMemorySize, LDS_BYTES);
  k_fused<<<NWG, 512, LDS_BYTES, stream>>>((const u16*)d_in[0], (const u16*)d_in[1], (const u16*)d_in[2], (const u16*)d_in[3], (const u16*)d_in[4],
                                           (const u16*)d_in[6], (const u16*)d_in[7], (const u16*)d_in[8], (u16*)d_out, (unsigned char*)d_ws);
}
```

```cpp
#include <hip/hip_runtime.h>
#include <hip/hip_bf16.h>
#include <stdint.h>

#define GAS __attribute__((address_space(1)))
#define LAS __attribute__((address_space(3)))
typedef unsigned v4u __attribute__((ext_vector_type(4)));
typedef unsigned v2u __attribute__((ext_vector_type(2)));
typedef float v4f __attribute__((ext_vector_type(4)));
typedef float v2f __attribute__((ext_vector_type(2)));
typedef __bf16 v8bf __attribute__((ext_vector_type(8)));
typedef __bf16 v2bf __attribute__((ext_vector_type(2)));
typedef short v4s __attribute__((ext_vector_type(4)));
typedef short v8s __attribute__((ext_vector_type(8)));
typedef unsigned short u16;
typedef unsigned long long u64;

constexpr int DM = 512, SK = 32768, FF = 1792, NWG = 256;
constexpr float SCALE = 0.088388347648318440f, LOG2E = 1.4426950408889634f;
constexpr int N0 = 31, N1 = 33, N2 = 31, N3 = 33;
static_assert(N0 + N1 + N2 + N3 == SK / 16 / 16, "4 waves x 4 workgroups x (N0 + .. + N3) = 2048 tiles of 16 keys per kv group");
constexpr int T_Q = 250, T_DOWN = 1000, T_Z = 1800;
#define RB 120

constexpr int L_RING = 0, L_STAGE = 4 * 4 * 8192, L_XN = L_STAGE + 16384, L_QL = L_XN + 2048, L_MISC = L_QL + 1024, L_H = L_MISC + 256, L_OP = L_H + 1024, L_QP = L_OP + 256,
              LDS_BYTES = L_QP + 2048;
constexpr unsigned W_EPOCH = 0, W_ALIVE = 4096, W_QG = W_ALIVE + 2048, W_HG = W_QG + 2 * 4096 * 8, W_ML = W_HG + 4 * FF * 8, W_IN1 = W_ML + 16 * 1024,
                   W_INY = W_IN1 + NWG * 4096, W_INZ = W_INY + 64 * 16384, W_END = W_INZ + 64 * 2048;
constexpr unsigned SPIN_CAP = 1u << 15;

__device__ __forceinline__ float rbf(float f) { __bf16 b = (__bf16)f; return __uint_as_float((unsigned)__builtin_bit_cast(u16, b) << 16); }
__device__ __forceinline__ unsigned pk2(float lo, float hi) { v2bf p = {(__bf16)lo, (__bf16)hi}; return __builtin_bit_cast(unsigned, p); }
__device__ __forceinline__ float lo16(unsigned v) { return __uint_as_float(v << 16); }
__device__ __forceinline__ float hi16(unsigned v) { return __uint_as_float(v & 0xffff0000u); }
template <int CTRL> __device__ __forceinline__ float dpp(float x) {
  return __builtin_bit_cast(float, __builtin_amdgcn_mov_dpp(__builtin_bit_cast(int, x), CTRL, 0xf, 0xf, true));
}
constexpr int XOR1 = 0xB1, XOR2 = 0x4E, HMIR = 0x141, MIR = 0x140, ROR8 = 0x128;
__device__ __forceinline__ float swap32_add(float a, float b) { auto s = __builtin_amdgcn_permlane32_swap(__float_as_uint(a), __float_as_uint(b), false, false); return __uint_as_float(s[0]) + __uint_as_float(s[1]); }
__device__ __forceinline__ float swap16_add(float a, float b) { auto s = __builtin_amdgcn_permlane16_swap(__float_as_uint(a), __float_as_uint(b), false, false); return __uint_as_float(s[0]) + __uint_as_float(s[1]); }
__device__ __forceinline__ float xrow16_max(float x) {
  auto s = __builtin_amdgcn_permlane16_swap(__float_as_uint(x), __float_as_uint(x), false, false);
  x = fmaxf(__uint_as_float(s[0]), __uint_as_float(s[1]));
  auto t = __builtin_amdgcn_permlane32_swap(__float_as_uint(x), __float_as_uint(x), false, false);
  return fmaxf(__uint_as_float(t[0]), __uint_as_float(t[1]));
}
__device__ __forceinline__ float xrow16_sum(float x) { x = swap16_add(x, x); return swap32_add(x, x); }
__device__ __forceinline__ float row_sum(float x) { x += dpp<XOR1>(x); x += dpp<XOR2>(x); x += dpp<HMIR>(x); x += dpp<MIR>(x); return x; }
__device__ __forceinline__ float row_max(float x) { x = fmaxf(x, dpp<XOR1>(x)); x = fmaxf(x, dpp<XOR2>(x)); x = fmaxf(x, dpp<HMIR>(x)); x = fmaxf(x, dpp<MIR>(x)); return x; }
__device__ __forceinline__ float wave_sum(float x) { return xrow16_sum(row_sum(x)); }
__device__ __forceinline__ float silu_f(float x) { return x * __builtin_amdgcn_rcpf(1.f + __builtin_amdgcn_exp2f(-x * LOG2E)); }
__device__ __forceinline__ float dot2(unsigned a, unsigned b, float c) { return __builtin_amdgcn_fdot2_f32_bf16(__builtin_bit_cast(v2bf, a), __builtin_bit_cast(v2bf, b), c, false); }

struct Tag { unsigned lo, hi; };
__device__ __forceinline__ Tag make_tag(v2u e) { const u64 t = (((u64)e.y << 32) | e.x) + 1; return Tag{(unsigned)(t & 0xff) << 24, (unsigned)(t >> 8)}; }
__device__ __forceinline__ v4u gran2(float a, float b, Tag t) { return v4u{(__float_as_uint(a) >> 8) | t.lo, t.hi, (__float_as_uint(b) >> 8) | t.lo, t.hi}; }
__device__ __forceinline__ bool gok2(v4u v, Tag t) { return (v.y == t.hi) & (v.w == t.hi) & (((v.x ^ t.lo) | (v.z ^ t.lo)) < 0x1000000u); }
__device__ __forceinline__ float gval(unsigned lo) { return __uint_as_float(lo << 8); }
#define BLOAD16(off) __builtin_amdgcn_raw_buffer_load_b128(rs, (off), 0, 16)
#define BSTORE16(v, off) __builtin_amdgcn_raw_buffer_store_b128((v), rs, (off), 0, 16)
#define MEMFENCE() asm volatile("" ::: "memory")
#define LGKM0() asm volatile("s_waitcnt lgkmcnt(0)" ::: "memory")
#define VMCNT(n) asm volatile("s_waitcnt vmcnt(" #n ")" ::: "memory")
#define BARRIER() do { LGKM0(); __builtin_amdgcn_s_barrier(); MEMFENCE(); } while (0)
#define LDSP(T, a) ((LAS T*)(uintptr_t)(a))
#define LDS_SPIN(cond) do { for (unsigned spin_ = 0; spin_ < (SPIN_CAP << 7); ++spin_) { if (cond) break; __builtin_amdgcn_s_sleep(1); } MEMFENCE(); } while (0)

__device__ __forceinline__ void dma1(const void* gsrc, unsigned lds_dst) {
  unsigned keep;
  asm volatile("s_mov_b32 %0, m0\n\ts_mov_b32 m0, %2\n\ts_nop 0\n\tglobal_load_lds_dwordx4 %1, off nt\n\ts_mov_b32 m0, %0" : "=&s"(keep) : "v"(gsrc), "s"(lds_dst) : "memory");
}
__device__ __forceinline__ void dma4(unsigned oe, unsigned oo, const char* gb, unsigned lds_dst) {
  unsigned keep;
  asm volatile("s_mov_b32 %0, m0\n\ts_mov_b32 m0, %4\n\ts_nop 0\n\t"
               "global_load_lds_dwordx4 %1, %3 nt\n\tglobal_load_lds_dwordx4 %2, %3 offset:1024 nt\n\t"
               "global_load_lds_dwordx4 %1, %3 offset:2048 nt\n\tglobal_load_lds_dwordx4 %2, %3 offset:3072 nt\n\ts_mov_b32 m0, %0"
               : "=&s"(keep) : "v"(oe), "v"(oo), "s"(gb), "s"(lds_dst) : "memory");
}
template <int R> __device__ __forceinline__ void park4(unsigned oe, unsigned oo, const char* gb) {
  asm volatile("global_load_dwordx4 v[%c3+0:%c3+3], %0, %2 nt\n\tglobal_load_dwordx4 v[%c3+4:%c3+7], %1, %2 offset:1024 nt\n\t"
               "global_load_dwordx4 v[%c3+8:%c3+11], %0, %2 offset:2048 nt\n\tglobal_load_dwordx4 v[%c3+12:%c3+15], %1, %2 offset:3072 nt"
               :: "v"(oe), "v"(oo), "s"(gb), "i"(R) : "memory");
}
template <int R> __device__ __forceinline__ void unpark(unsigned addr) {
  asm volatile("ds_write_b128 %0, v[%c1+0:%c1+3]\n\tds_write_b128 %0, v[%c1+4:%c1+7] offset:1024\n\tds_write_b128 %0, v[%c1+8:%c1+11] offset:2048\n\t"
               "ds_write_b128 %0, v[%c1+12:%c1+15] offset:3072\n\tds_write_b128 %0, v[%c1+16:%c1+19] offset:4096\n\tds_write_b128 %0, v[%c1+20:%c1+23] offset:5120\n\t"
               "ds_write_b128 %0, v[%c1+24:%c1+27] offset:6144\n\tds_write_b128 %0, v[%c1+28:%c1+31] offset:7168\n\ts_waitcnt lgkmcnt(0)"
               :: "v"(addr), "i"(R) : "memory");
}
__device__ __forceinline__ v4f mfma_tr(unsigned a_lo, unsigned a_hi, v8s bb, v4f acc) {
  const v4s a0 = __builtin_amdgcn_ds_read_tr16_b64_v4i16(LDSP(v4s, a_lo));
  const v4s a1 = __builtin_amdgcn_ds_read_tr16_b64_v4i16(LDSP(v4s, a_hi));
  const v8s a = __builtin_shufflevector(a0, a1, 0, 1, 2, 3, 4, 5, 6, 7);
  return __builtin_amdgcn_mfma_f32_16x16x32_bf16(__builtin_bit_cast(v8bf, a), __builtin_bit_cast(v8bf, bb), acc, 0, 0, 0);
}

__global__ void __launch_bounds__(512, 2) k_fused(const u16* __restrict__ x, const u16* __restrict__ Kc, const u16* __restrict__ Vc, const u16* __restrict__ wq,
                                                  const u16* __restrict__ wo, const u16* __restrict__ w1, const u16* __restrict__ w3, const u16* __restrict__ w2,
                                                  u16* __restrict__ out, unsigned char* __restrict__ ws) {
  extern __shared__ __attribute__((aligned(16))) unsigned char lds[];
  const int wave = __builtin_amdgcn_readfirstlane(threadIdx.x >> 6);
  const int wg = blockIdx.x, grp = wg >> 4, sp = wg & 15, b = grp >> 3, kvh = grp & 7;
  const int job = (sp >> 2) ^ (grp & 1), jidx = grp * 4 + (sp & 3);
  const bool pj = job == 0 || (job == 2 && jidx < 56);
  const unsigned ldsbase = (unsigned)(uintptr_t)(LAS unsigned char*)lds;
  const __amdgpu_buffer_rsrc_t rs = __builtin_amdgcn_make_buffer_rsrc(ws, 0, W_END, 0x00020000);

  if (wave < 4) {
    asm volatile("; SPATH_BEGIN" ::: "v247");
    int lane;
    asm volatile("v_mbcnt_lo_u32_b32 %0, -1, 0\n\tv_mbcnt_hi_u32_b32 %0, -1, %0" : "=v"(lane));
    const int n = lane & 15, g = lane >> 4;
    const unsigned ring = ldsbase + L_RING + wave * 32768;
    const unsigned koe = g * 256 + ((n & 8) | ((n & 7) ^ g)) * 16, koo = g * 256 + ((n & 8) | ((n & 7) ^ (4 + g))) * 16;
    const unsigned voe = g * 256 + (n ^ (g << 1)) * 16, voo = g * 256 + (n ^ ((4 + g) << 1)) * 16;
    const int gx = grp & 1, qd = sp >> 2;
    const int c0 = gx ? N1 : N0, c1 = gx ? N0 : N1, c2 = gx ? N3 : N2;
    const int nt = job == 0 ? N0 : job == 1 ? N1 : job == 2 ? N2 : N3;
    const int t0s = 4 * ((qd > 0 ? c0 : 0) + (qd > 1 ? c1 : 0) + (qd > 2 ? c2 : 0)) + (sp & 3) * nt;
    const size_t gbase = (size_t)grp * SK * 256 + (size_t)wave * 4096 + (size_t)t0s * 16384;
    const char* kptr = (const char*)Kc + gbase;
    const char* vptr = (const char*)Vc + gbase;
#define ISSUE_K(t, slot) dma4(koe, koo, kptr + (size_t)(t) * 16384, ring + (slot) * 8192)
#define ISSUE_V(t, slot) dma4(voe, voo, vptr + (size_t)(t) * 16384, ring + (slot) * 8192 + 4096)
#define PARK(r) do { VMCNT(4); park4<RB + 32 * (r)>(koe, koo, kptr + (size_t)(4 + (r)) * 16384); VMCNT(4); park4<RB + 32 * (r) + 16>(voe, voo, vptr + (size_t)(4 + (r)) * 16384); } while (0)
    if (pj) { __builtin_amdgcn_s_barrier(); LDS_SPIN(*LDSP(volatile unsigned, ldsbase + L_MISC + 32 + (wave ? wave : 1) * 4)); }
    ISSUE_K(0, 0); ISSUE_V(0, 0);
    if (!pj) __builtin_amdgcn_s_barrier();
    VMCNT(4); ISSUE_K(1, 1); VMCNT(4); ISSUE_V(1, 1);
    VMCNT(4); ISSUE_K(2, 2); VMCNT(4); ISSUE_V(2, 2);
    VMCNT(4); ISSUE_K(3, 3); VMCNT(4); ISSUE_V(3, 3);
    PARK(0); PARK(1); PARK(2); PARK(3);
    unsigned ka[4], va[8];
#pragma unroll
    for (int t = 0; t < 4; ++t) ka[t] = ring + n * 256 + ((8 * (g & 1)) | ((4 * (g >> 1) + t) ^ (n & 7))) * 16;
    { const unsigned row = lane >> 2;
#pragma unroll
      for (int dt = 0; dt < 8; ++dt) va[dt] = ring + 4096 + row * 256 + ((dt ^ (row & 7)) << 5) + (lane & 3) * 8; }
    const unsigned wa2 = ring + 2 * 8192 + lane * 16, wa3 = wa2 + 8192;
    LDS_SPIN(*LDSP(volatile unsigned, ldsbase + L_MISC + 16));
    v8bf qf[4];
#pragma unroll
    for (int t = 0; t < 4; ++t) qf[t] = __builtin_bit_cast(v8bf, *LDSP(const v4u, ldsbase + L_QL + (n & 3) * 256 + (8 * (g & 1) + 4 * (g >> 1) + t) * 16));
    float m = -INFINITY, l = 0.f;
    v4f O[8];
#pragma unroll
    for (int dt = 0; dt < 8; ++dt) O[dt] = v4f{0, 0, 0, 0};
    for (int j = 0; j < nt; ++j) {
      int slot = j & 3;
      if (j >= 8) {
        if (j + 1 < nt) VMCNT(8); else VMCNT(0);
        if (j + 2 < nt) { ISSUE_K(j + 2, (j + 2) & 3); ISSUE_V(j + 2, (j + 2) & 3); }
      } else if (j >= 2) {
        slot = 2 + (j & 1);
        if (j == 2) { VMCNT(8); ISSUE_K(8, 0); ISSUE_V(8, 0); }
        else if (j == 4) unpark<RB>(wa2);
        else if (j == 5) unpark<RB + 32>(wa3);
        else if (j == 6) { VMCNT(8); ISSUE_K(9, 1); ISSUE_V(9, 1); unpark<RB + 64>(wa2); }
        else if (j == 7) unpark<RB + 96>(wa3);
      }
      if (j == nt - 3 && wave == 0 && lane == 0) *LDSP(volatile unsigned, ldsbase + L_MISC + 28) = 1u;
      const unsigned so = slot * 8192;
      v4u kf[4]; v4s vf[8];
#pragma unroll
      for (int t = 0; t < 4; ++t) kf[t] = *LDSP(const v4u, ka[t] + so);
#pragma unroll
      for (int dt = 0; dt < 8; ++dt) vf[dt] = __builtin_amdgcn_ds_read_tr16_b64_v4i16(LDSP(v4s, va[dt] + so));
      v4f S = {0, 0, 0, 0};
#pragma unroll
      for (int t = 0; t < 4; ++t) S = __builtin_amdgcn_mfma_f32_16x16x32_bf16(__builtin_bit_cast(v8bf, kf[t]), qf[t], S, 0, 0, 0);
      const unsigned a01 = pk2(S[0], S[1]), a23 = pk2(S[2], S[3]);
      const unsigned c01 = pk2(lo16(a01) * SCALE, hi16(a01) * SCALE), c23 = pk2(lo16(a23) * SCALE, hi16(a23) * SCALE);
      const float s0 = lo16(c01), s1 = hi16(c01), s2 = lo16(c23), s3 = hi16(c23);
      const float tm = xrow16_max(fmaxf(fmaxf(s0, s1), fmaxf(s2, s3)));
      if (__any(tm > m)) {
        const float mn = fmaxf(m, tm);
        const float alpha = __builtin_amdgcn_exp2f((m - mn) * LOG2E);
        l *= alpha;
#pragma unroll
        for (int dt = 0; dt < 8; ++dt) O[dt] *= alpha;
        m = mn;
      }
      const float p0 = __builtin_amdgcn_exp2f((s0 - m) * LOG2E), p1 = __builtin_amdgcn_exp2f((s1 - m) * LOG2E);
      const float p2 = __builtin_amdgcn_exp2f((s2 - m) * LOG2E), p3 = __builtin_amdgcn_exp2f((s3 - m) * LOG2E);
      l += (p0 + p1) + (p2 + p3);
      const v2u pp = {pk2(p0, p1), pk2(p2, p3)};
      const v4s pf = __builtin_bit_cast(v4s, pp);
#pragma unroll
      for (int dt = 0; dt < 8; ++dt) O[dt] = __builtin_amdgcn_mfma_f32_16x16x16bf16_1k(vf[dt], pf, O[dt], 0, 0, 0);
      LGKM0();
    }
    l = xrow16_sum(l);
    if (n < 4) {
      const unsigned r = ring + n * 528;
#pragma unroll
      for (int dt = 0; dt < 8; ++dt) *LDSP(v4f, r + 16 + (16 * dt + 4 * g) * 4) = O[dt];
      if (g == 0) *LDSP(v2f, r) = v2f{m, l};
    }
    BARRIER();
    {
      const v2u tg = *LDSP(const v2u, ldsbase + L_MISC);
      const Tag T{tg.x, tg.y};
      v2f ml[4], ov[4];
#pragma unroll
      for (int w = 0; w < 4; ++w) { const unsigned r = ldsbase + L_RING + w * 32768 + wave * 528; ml[w] = *LDSP(const v2f, r); ov[w] = *LDSP(const v2f, r + 16 + lane * 8); }
      const float M = fmaxf(fmaxf(ml[0].x, ml[1].x), fmaxf(ml[2].x, ml[3].x));
      float L = 0, o0 = 0, o1 = 0;
#pragma unroll
      for (int w = 0; w < 4; ++w) { const float wt = __builtin_amdgcn_exp2f((ml[w].x - M) * LOG2E); L = fmaf(wt, ml[w].y, L); o0 = fmaf(wt, ov[w].x, o0); o1 = fmaf(wt, ov[w].y, o1); }
      BSTORE16(gran2(o0, o1, T), W_IN1 + ((grp * 16 + 4 * wave + g) * 16 + sp) * 256 + n * 16);
      if (lane == 0) BSTORE16(gran2(M, L, T), W_ML + (grp * 16 + sp) * 64 + wave * 16);
    }
    asm volatile("; SPATH_END");
    return;
  }

  const int lane = threadIdx.x & 63, n = lane & 15, g = lane >> 4, hw = wave - 4;
  const u64 tk0 = __builtin_amdgcn_s_memrealtime();
#define WAIT_UNTIL(ticks) do { while (__builtin_amdgcn_s_memrealtime() - tk0 < (u64)(ticks)) __builtin_amdgcn_s_sleep(16); } while (0)
  Tag T;
  v4u w[28];
  {
    v2u ep;
    const unsigned char* e = ws + W_EPOCH;
    if (pj) {
      const int mat = jidx >= 28, cbk = jidx - 28 * mat;
      const char* wsrc = job == 0 ? (const char*)wq + jidx * 128 : (const char*)(mat ? w3 : w1) + cbk * 128;
      const unsigned ldw = job == 0 ? 8192u : (unsigned)(FF * 2);
      const unsigned obase = job == 0 ? W_QG + 64 * jidx * 8 : W_HG + (mat * 2 * FF + 64 * cbk) * 8, ostride = job == 0 ? 4096u * 8 : (unsigned)(FF * 8);
      const unsigned stg = hw == 0 ? ldsbase + L_STAGE : ldsbase + L_RING + hw * 32768 + 16384;
      v4u x0, x1;
      { const u16* xp = x + lane * 8;
        asm volatile("global_load_dwordx4 %0, %3, off nt\n\tglobal_load_dwordx4 %1, %3, off offset:1024 nt\n\tglobal_load_dwordx2 %2, %4, off nt" : "=&v"(x0), "=&v"(x1), "=&v"(ep) : "v"(xp), "v"(e) : "memory"); }
      { const char* src = wsrc + (size_t)(128 * hw + (lane >> 3)) * ldw + (lane & 7) * 16;
#pragma unroll
        for (int i = 0; i < 16; ++i) dma1(src + (size_t)i * (8 * ldw), stg + i * 1024); }
      if (hw == 0 && lane < 3) *LDSP(v4u, ldsbase + L_MISC + 16 + lane * 16) = v4u{0, 0, 0, 0};
      BARRIER();
      asm volatile("s_waitcnt vmcnt(16)" : "+v"(x0), "+v"(x1), "+v"(ep) :: "memory");
      {
        float v[2][8];
#pragma unroll
        for (int j = 0; j < 4; ++j) { v[0][2 * j] = lo16(x0[j]); v[0][2 * j + 1] = hi16(x0[j]); v[1][2 * j] = lo16(x1[j]); v[1][2 * j + 1] = hi16(x1[j]); }
#pragma unroll
        for (int bb = 0; bb < 2; ++bb) {
          const float s_ = ((v[bb][0] + v[bb][1]) + (v[bb][2] + v[bb][3])) + ((v[bb][4] + v[bb][5]) + (v[bb][6] + v[bb][7]));
          const float mean = wave_sum(s_) * (1.f / DM);
          float q_ = 0;
#pragma unroll
          for (int j = 0; j < 8; ++j) { const float d = v[bb][j] - mean; q_ = fmaf(d, d, q_); }
          const float var = wave_sum(q_) * (1.f / DM);
          const float mb = rbf(mean);
          const float inv = __builtin_amdgcn_rcpf(rbf(__builtin_amdgcn_sqrtf(rbf(rbf(var) + 1e-5f))));
          v4u o;
#pragma unroll
          for (int j = 0; j < 4; ++j) o[j] = pk2(rbf(v[bb][2 * j] - mb) * inv, rbf(v[bb][2 * j + 1] - mb) * inv);
          const unsigned xa = ldsbase + L_XN + bb * 1024 + (lane >> 2) * 64 + (lane & 1) * 32 + ((lane >> 1) & 1) * 8;
          if (g == hw) { *LDSP(v2u, xa) = v2u{o[0], o[1]}; *LDSP(v2u, xa + 16) = v2u{o[2], o[3]}; }
        }
      }
      asm volatile("s_waitcnt vmcnt(0) lgkmcnt(0)" ::: "memory");
      v4f acc[4];
#pragma unroll
      for (int ct = 0; ct < 4; ++ct) acc[ct] = v4f{0, 0, 0, 0};
      { const unsigned aaddr = stg + (lane >> 2) * 128 + (lane & 3) * 8, baddr = ldsbase + L_XN + (lane & 1) * 1024 + hw * 256 + g * 16;
#pragma unroll
        for (int s_ = 0; s_ < 4; ++s_) { const v8s bb = *LDSP(const v8s, baddr + s_ * 64);
#pragma unroll
          for (int ct = 0; ct < 4; ++ct) acc[ct] = mfma_tr(aaddr + s_ * 4096 + ct * 32, aaddr + s_ * 4096 + 2048 + ct * 32, bb, acc[ct]); } }
      if (n < 2) {
#pragma unroll
        for (int ct = 0; ct < 4; ++ct) *LDSP(v4f, ldsbase + L_QP + hw * 512 + (ct * 8 + g * 2 + n) * 16) = acc[ct];
      }
      LGKM0();
      if (hw != 0) {
        if (lane == 0) *LDSP(volatile unsigned, ldsbase + L_MISC + 32 + hw * 4) = 1u;
      } else {
        T = make_tag(ep);
        for (unsigned spin = 0; spin < (SPIN_CAP << 7); ++spin) { const v4u f = *LDSP(volatile v4u, ldsbase + L_MISC + 32); if (f.y & f.z & f.w) break; }
        MEMFENCE();
        if (n < 8) {
          const unsigned qa = ldsbase + L_QP + ((n >> 1) * 8 + g * 2 + (n & 1)) * 16;
          const v4f q = (*LDSP(const v4f, qa) + *LDSP(const v4f, qa + 512)) + (*LDSP(const v4f, qa + 1024) + *LDSP(const v4f, qa + 1536));
          const unsigned a = obase + (n & 1) * ostride + (16 * (n >> 1) + 4 * g) * 8;
          BSTORE16(gran2(rbf(q[0]), rbf(q[1]), T), a);
          BSTORE16(gran2(rbf(q[2]), rbf(q[3]), T), a + 16);
        }
      }
    } else {
      if (hw == 0) {
        asm volatile("global_load_dwordx2 %0, %1, off nt" : "=&v"(ep) : "v"(e) : "memory");
        if (lane < 3) *LDSP(v4u, ldsbase + L_MISC + 16 + lane * 16) = v4u{0, 0, 0, 0};
      }
      BARRIER();
      if (hw == 2 && job == 1) {
#pragma unroll
        for (int r = 0; r < 28; ++r) w[r] = __builtin_nontemporal_load((const GAS v4u*)(w2 + (size_t)((jidx >> 2) * 112 + g * 28 + r) * DM + (jidx & 3) * 128 + n * 8));
      }
      if (hw == 0) {
        asm volatile("s_waitcnt vmcnt(0)" : "+v"(ep) :: "memory");
        T = make_tag(ep);
        WAIT_UNTIL(T_Q);
      }
    }
    if (hw == 0) {
      if (lane == 0) __builtin_amdgcn_raw_buffer_store_b64(v2u{T.lo, T.hi}, rs, W_ALIVE + wg * 8, 0, 16);
      const unsigned a = W_QG + (b * 4096 + kvh * 512) * 8 + lane * 16;
      v4u v[4];
      bool ok[4] = {false, false, false, false};
      for (unsigned spin = 0; spin < SPIN_CAP; ++spin) {
        MEMFENCE();
#pragma unroll
        for (int i = 0; i < 4; ++i) if (!ok[i]) v[i] = BLOAD16(a + i * 1024);
#pragma unroll
        for (int i = 0; i < 4; ++i) ok[i] = gok2(v[i], T);
        if (__all(ok[0] & ok[1] & ok[2] & ok[3])) break;
        __builtin_amdgcn_s_sleep(4);
      }
#pragma unroll
      for (int i = 0; i < 4; ++i) *LDSP(unsigned, ldsbase + L_QL + i * 256 + lane * 4) = ((v[i].x >> 8) & 0xffffu) | ((v[i].z << 8) & 0xffff0000u);
      if (lane == 0) *LDSP(v2u, ldsbase + L_MISC) = v2u{T.lo, T.hi};
      LGKM0();
      if (lane == 0) *LDSP(volatile unsigned, ldsbase + L_MISC + 16) = 1u;
    } else {
      LDS_SPIN(*LDSP(volatile unsigned, ldsbase + L_MISC + 16));
      const v2u tg = *LDSP(const v2u, ldsbase + L_MISC); T = Tag{tg.x, tg.y};
    }
  }
  if (hw == 2 && job == 1) {
    const int rb = jidx >> 2, cb = jidx & 3;
    const unsigned a = W_HG + (rb * 112 + 2 * (lane < 56 ? lane : 0)) * 8;
    v4u pv[4];
    WAIT_UNTIL(T_DOWN);
    for (unsigned spin = 0; spin < SPIN_CAP; ++spin) {
      if (spin) __builtin_amdgcn_s_sleep(64);
      MEMFENCE();
#pragma unroll
      for (int i = 0; i < 4; ++i) pv[i] = BLOAD16(a + i * FF * 8);
      if (__all(gok2(pv[0], T) & gok2(pv[1], T) & gok2(pv[2], T) & gok2(pv[3], T))) break;
    }
    if (lane < 56) {
#pragma unroll
      for (int bb = 0; bb < 2; ++bb)
        *LDSP(v2f, ldsbase + L_H + bb * 512 + lane * 8) = v2f{rbf(rbf(silu_f(gval(pv[bb].x))) * gval(pv[2 + bb].x)), rbf(rbf(silu_f(gval(pv[bb].z))) * gval(pv[2 + bb].z))};
    }
    LGKM0();
    float z[2][8];
#pragma unroll
    for (int c = 0; c < 8; ++c) z[0][c] = z[1][c] = 0.f;
#pragma unroll
    for (int ch = 0; ch < 7; ++ch) {
      const v4f h0 = *LDSP(const v4f, ldsbase + L_H + (g * 28 + ch * 4) * 4), h1 = *LDSP(const v4f, ldsbase + L_H + 512 + (g * 28 + ch * 4) * 4);
#pragma unroll
      for (int r = 0; r < 4; ++r)
#pragma unroll
        for (int j = 0; j < 4; ++j) { const float wl = lo16(w[ch * 4 + r][j]), wh = hi16(w[ch * 4 + r][j]);
          z[0][2 * j] = fmaf(h0[r], wl, z[0][2 * j]); z[0][2 * j + 1] = fmaf(h0[r], wh, z[0][2 * j + 1]);
          z[1][2 * j] = fmaf(h1[r], wl, z[1][2 * j]); z[1][2 * j + 1] = fmaf(h1[r], wh, z[1][2 * j + 1]); }
    }
#pragma unroll
    for (int bb = 0; bb < 2; ++bb) {
      float t4[4];
#pragma unroll
      for (int j = 0; j < 4; ++j) t4[j] = swap32_add(z[bb][j], z[bb][j + 4]);
      BSTORE16(gran2(swap16_add(t4[0], t4[2]), swap16_add(t4[1], t4[3]), T), W_INZ + ((bb * 32 + 8 * cb + (n >> 1)) * 16 + rb) * 128 + ((n & 1) * 4 + g) * 16);
    }
  }
  if (hw == 3 && wg == 0) {
    WAIT_UNTIL(T_DOWN);
    for (unsigned spin = 0; spin < SPIN_CAP; ++spin) {
      if (spin) __builtin_amdgcn_s_sleep(64);
      MEMFENCE();
      const v4u a0 = BLOAD16(W_ALIVE + lane * 16), a1 = BLOAD16(W_ALIVE + 1024 + lane * 16);
      if (__all(gok2(a0, T) & gok2(a1, T))) break;
    }
    if (lane == 0) {
      const v2u e = __builtin_amdgcn_raw_buffer_load_b64(rs, W_EPOCH, 0, 16);
      const u64 tv = (((u64)e.y << 32) | e.x) + 1;
      __builtin_amdgcn_raw_buffer_store_b64(v2u{(unsigned)tv, (unsigned)(tv >> 32)}, rs, W_EPOCH, 0, 16);
    }
  }
  float xr0 = 0.f, xr1 = 0.f;
  if (job == 3) {
    const unsigned xv = __builtin_nontemporal_load((const GAS unsigned*)(x + jidx * 16 + (lane & 7) * 2));
    xr0 = lo16(xv); xr1 = hi16(xv);
    if (hw == 3) {
      const unsigned a = W_INZ + jidx * 2048 + lane * 16;
      v4u v[2];
      WAIT_UNTIL(T_Z);
      for (unsigned spin = 0; spin < SPIN_CAP; ++spin) {
        if (spin) __builtin_amdgcn_s_sleep(64);
        MEMFENCE();
        v[0] = BLOAD16(a); v[1] = BLOAD16(a + 1024);
        if (__all(gok2(v[0], T) & gok2(v[1], T))) break;
      }
      float z0 = gval(v[0].x) + gval(v[1].x), z1 = gval(v[0].z) + gval(v[1].z);
      z0 += dpp<ROR8>(z0); z1 += dpp<ROR8>(z1);
      z0 = xrow16_sum(z0); z1 = xrow16_sum(z1);
      if (lane < 8) *LDSP(v2f, ldsbase + L_H + lane * 8) = v2f{rbf(silu_f(rbf(z0))), rbf(silu_f(rbf(z1)))};
    }
  }
  unsigned wp[4][8];
  {
    const int row0 = (kvh * 4 + (sp >> 2)) * 128 + (sp & 3) * 32 + 8 * g;
    v4u w_[8];
    for (unsigned spin = 0; spin < (SPIN_CAP << 7); ++spin) { if (*LDSP(volatile unsigned, ldsbase + L_MISC + 28)) break; __builtin_amdgcn_s_sleep(8); }
    MEMFENCE();
#pragma unroll
    for (int i = 0; i < 8; ++i) w_[i] = __builtin_nontemporal_load((const GAS v4u*)(wo + (size_t)(row0 + i) * DM + 128 * hw + 8 * n));
#pragma unroll
    for (int k = 0; k < 4; ++k)
#pragma unroll
      for (int j = 0; j < 4; ++j) { wp[k][2 * j] = __builtin_amdgcn_perm(w_[2 * k + 1][j], w_[2 * k][j], 0x05040100u); wp[k][2 * j + 1] = __builtin_amdgcn_perm(w_[2 * k + 1][j], w_[2 * k][j], 0x07060302u); }
#pragma unroll
    for (int k = 0; k < 4; ++k)
#pragma unroll
      for (int j = 0; j < 8; ++j) asm volatile("" : "+v"(wp[k][j]));
  }
  BARRIER();
  {
    unsigned op[4];
    if (hw == 0) {
      const unsigned a = W_IN1 + (wg * 16 + n) * 256 + g * 64, am = W_ML + (grp * 16 + n) * 64 + (sp >> 2) * 16;
      v4u v[4], ml;
      bool ok = false, mok = false, eager = false;
      for (unsigned spin = 0; spin < SPIN_CAP; ++spin) {
        MEMFENCE();
        const bool ld = !ok & (mok | eager);
        if (!mok) ml = BLOAD16(am);
        if (ld) {
#pragma unroll
          for (int i = 0; i < 4; ++i) v[i] = BLOAD16(a + i * 16);
          ok = gok2(v[0], T) & gok2(v[1], T) & gok2(v[2], T) & gok2(v[3], T);
        }
        mok = gok2(ml, T);
        const u64 miss = __ballot(!mok);
        if (__all(ok & mok)) break;
        eager = __builtin_popcountll(miss) <= 8;
        if (!eager) __builtin_amdgcn_s_sleep(3);
      }
      const float mp = gval(ml.x), lp = gval(ml.z);
      const float M = row_max(mp);
      const float wt = __builtin_amdgcn_exp2f((mp - M) * LOG2E);
      const float invL = __builtin_amdgcn_rcpf(row_sum(wt * lp));
#pragma unroll
      for (int i = 0; i < 4; ++i) op[i] = pk2(row_sum(wt * gval(v[i].x)) * invL, row_sum(wt * gval(v[i].z)) * invL);
      if (n == 0) *LDSP(v4u, ldsbase + L_OP + g * 16) = v4u{op[0], op[1], op[2], op[3]};
      LGKM0();
      if (lane == 0) *LDSP(volatile unsigned, ldsbase + L_MISC + 20) = 1u;
    } else {
      LDS_SPIN(*LDSP(volatile unsigned, ldsbase + L_MISC + 20));
      const v4u o4 = *LDSP(const v4u, ldsbase + L_OP + g * 16);
      op[0] = o4.x; op[1] = o4.y; op[2] = o4.z; op[3] = o4.w;
    }
    float y[8];
#pragma unroll
    for (int c = 0; c < 8; ++c) { float acc = 0.f;
#pragma unroll
      for (int k = 0; k < 4; ++k) acc = dot2(wp[k][c], op[k], acc);
      y[c] = acc; }
    float t4[4];
#pragma unroll
    for (int j = 0; j < 4; ++j) t4[j] = swap32_add(y[j], y[j + 4]);
    BSTORE16(gran2(swap16_add(t4[0], t4[2]), swap16_add(t4[1], t4[3]), T), W_INY + ((b * 32 + 8 * hw + (n >> 1)) * 128 + (wg & 127)) * 128 + ((n & 1) * 4 + g) * 16);
  }
  if (job == 3) {
    const unsigned a = W_INY + jidx * 16384 + hw * 4096 + lane * 16;
    v4u v[4];
    bool ok[4] = {false, false, false, false};
    for (unsigned spin = 0; spin < SPIN_CAP; ++spin) {
      MEMFENCE();
#pragma unroll
      for (int i = 0; i < 4; ++i) if (!ok[i]) v[i] = BLOAD16(a + i * 1024);
#pragma unroll
      for (int i = 0; i < 4; ++i) ok[i] = gok2(v[i], T);
      if (__all(ok[0] & ok[1] & ok[2] & ok[3])) break;
    }
    float s0 = (gval(v[0].x) + gval(v[1].x)) + (gval(v[2].x) + gval(v[3].x)), s1 = (gval(v[0].z) + gval(v[1].z)) + (gval(v[2].z) + gval(v[3].z));
    s0 += dpp<ROR8>(s0); s1 += dpp<ROR8>(s1);
    s0 = xrow16_sum(s0); s1 = xrow16_sum(s1);
    if (lane < 8) *LDSP(v2f, ldsbase + L_QP + hw * 64 + lane * 8) = v2f{s0, s1};
    LGKM0();
    unsigned old = 0;
    if (lane == 0) old = __hip_atomic_fetch_add(LDSP(unsigned, ldsbase + L_MISC + 24), 1u, __ATOMIC_RELAXED, __HIP_MEMORY_SCOPE_WORKGROUP);
    old = __builtin_amdgcn_readfirstlane(old);
    MEMFENCE();
    if (old == 3 && lane < 8) {
      const unsigned pa = ldsbase + L_QP + lane * 8;
      const v2f t = (*LDSP(const v2f, pa) + *LDSP(const v2f, pa + 64)) + (*LDSP(const v2f, pa + 128) + *LDSP(const v2f, pa + 192));
      const v2f gt = *LDSP(const v2f, ldsbase + L_H + lane * 8);
      *(GAS unsigned*)(out + jidx * 16 + lane * 2) = pk2(xr0 + rbf(rbf(t.x) * gt.x), xr1 + rbf(rbf(t.y) * gt.y));
    }
  }
}


__global__ void __launch_bounds__(64) k_tiny(unsigned char* ws) { if (ws == nullptr) *(volatile int*)0 = 0; }
__global__ void __launch_bounds__(512, 2) k_big(unsigned char* ws) {
  extern __shared__ unsigned char l2[];
  asm volatile("" ::: "v247");
  if (ws == nullptr) l2[threadIdx.x] = 1;
}

extern "C" void kernel_launch(void* const* d_in, const int* in_sizes, int n_in, void* d_out, int out_size, void* d_ws, size_t ws_size, hipStream_t stream) {
  (void)hipFuncSetAttribute((const void*)k_fused, hipFuncAttributeMaxDynamicSharedMemorySize, LDS_BYTES);
  (void)hipFuncSetAttribute((const void*)k_big, hipFuncAttributeMaxDynamicSharedMemorySize, LDS_BYTES);
  k_big<<<NWG, 512, 128 * 1024, stream>>>((unsigned char*)d_ws);
  k_fused<<<NWG, 512, LDS_BYTES, stream>>>((const u16*)d_in[0], (const u16*)d_in[1], (const u16*)d_in[2], (const u16*)d_in[3], (const u16*)d_in[4],
                                           (const u16*)d_in[6], (const u16*)d_in[7], (const u16*)d_in[8], (u16*)d_out, (unsigned char*)d_ws);
}
```

```cpp
#include <hip/hip_runtime.h>
#include <hip/hip_bf16.h>
#include <stdint.h>

#define GAS __attribute__((address_space(1)))
#define LAS __attribute__((address_space(3)))
typedef unsigned v4u __attribute__((ext_vector_type(4)));
typedef unsigned v2u __attribute__((ext_vector_type(2)));
typedef float v4f __attribute__((ext_vector_type(4)));
typedef float v2f __attribute__((ext_vector_type(2)));
typedef __bf16 v8bf __attribute__((ext_vector_type(8)));
typedef __bf16 v2bf __attribute__((ext_vector_type(2)));
typedef short v4s __attribute__((ext_vector_type(4)));
typedef short v8s __attribute__((ext_vector_type(8)));
typedef unsigned short u16;
typedef unsigned long long u64;

constexpr int DM = 512, SK = 32768, FF = 1792, NWG = 256;
constexpr float SCALE = 0.088388347648318440f, LOG2E = 1.4426950408889634f;
constexpr int N0 = 31, N1 = 33, N2 = 31, N3 = 33;
static_assert(N0 + N1 + N2 + N3 == SK / 16 / 16, "4 waves x 4 workgroups x (N0 + .. + N3) = 2048 tiles of 16 keys per kv group");
constexpr int T_Q = 250, T_DOWN = 1000, T_Z = 1800;
#define RB 120

constexpr int L_RING = 0, L_STAGE = 4 * 4 * 8192, L_XN = L_STAGE + 16384, L_QL = L_XN + 2048, L_MISC = L_QL + 1024, L_H = L_MISC + 256, L_OP = L_H + 1024, L_QP = L_OP + 256,
              LDS_BYTES = L_QP + 2048;
constexpr unsigned W_EPOCH = 0, W_ALIVE = 4096, W_QG = W_ALIVE + 2048, W_HG = W_QG + 2 * 4096 * 8, W_ML = W_HG + 4 * FF * 8, W_IN1 = W_ML + 16 * 1024,
                   W_INY = W_IN1 + NWG * 4096, W_INZ = W_INY + 64 * 16384, W_END = W_INZ + 64 * 2048;
constexpr unsigned SPIN_CAP = 1u << 15;

__device__ __forceinline__ float rbf(float f) { __bf16 b = (__bf16)f; return __uint_as_float((unsigned)__builtin_bit_cast(u16, b) << 16); }
__device__ __forceinline__ unsigned pk2(float lo, float hi) { v2bf p = {(__bf16)lo, (__bf16)hi}; return __builtin_bit_cast(unsigned, p); }
__device__ __forceinline__ float lo16(unsigned v) { return __uint_as_float(v << 16); }
__device__ __forceinline__ float hi16(unsigned v) { return __uint_as_float(v & 0xffff0000u); }
template <int CTRL> __device__ __forceinline__ float dpp(float x) {
  return __builtin_bit_cast(float, __builtin_amdgcn_mov_dpp(__builtin_bit_cast(int, x), CTRL, 0xf, 0xf, true));
}
constexpr int XOR1 = 0xB1, XOR2 = 0x4E, HMIR = 0x141, MIR = 0x140, ROR8 = 0x128;
__device__ __forceinline__ float swap32_add(float a, float b) { auto s = __builtin_amdgcn_permlane32_swap(__float_as_uint(a), __float_as_uint(b), false, false); return __uint_as_float(s[0]) + __uint_as_float(s[1]); }
__device__ __forceinline__ float swap16_add(float a, float b) { auto s = __builtin_amdgcn_permlane16_swap(__float_as_uint(a), __float_as_uint(b), false, false); return __uint_as_float(s[0]) + __uint_as_float(s[1]); }
__device__ __forceinline__ float xrow16_max(float x) {
  auto s = __builtin_amdgcn_permlane16_swap(__float_as_uint(x), __float_as_uint(x), false, false);
  x = fmaxf(__uint_as_float(s[0]), __uint_as_float(s[1]));
  auto t = __builtin_amdgcn_permlane32_swap(__float_as_uint(x), __float_as_uint(x), false, false);
  return fmaxf(__uint_as_float(t[0]), __uint_as_float(t[1]));
}
__device__ __forceinline__ float xrow16_sum(float x) { x = swap16_add(x, x); return swap32_add(x, x); }
__device__ __forceinline__ float row_sum(float x) { x += dpp<XOR1>(x); x += dpp<XOR2>(x); x += dpp<HMIR>(x); x += dpp<MIR>(x); return x; }
__device__ __forceinline__ float row_max(float x) { x = fmaxf(x, dpp<XOR1>(x)); x = fmaxf(x, dpp<XOR2>(x)); x = fmaxf(x, dpp<HMIR>(x)); x = fmaxf(x, dpp<MIR>(x)); return x; }
__device__ __forceinline__ float wave_sum(float x) { return xrow16_sum(row_sum(x)); }
__device__ __forceinline__ float silu_f(float x) { return x * __builtin_amdgcn_rcpf(1.f + __builtin_amdgcn_exp2f(-x * LOG2E)); }
__device__ __forceinline__ float dot2(unsigned a, unsigned b, float c) { return __builtin_amdgcn_fdot2_f32_bf16(__builtin_bit_cast(v2bf, a), __builtin_bit_cast(v2bf, b), c, false); }

struct Tag { unsigned lo, hi; };
__device__ __forceinline__ Tag make_tag(v2u e) { const u64 t = (((u64)e.y << 32) | e.x) + 1; return Tag{(unsigned)(t & 0xff) << 24, (unsigned)(t >> 8)}; }
__device__ __forceinline__ v4u gran2(float a, float b, Tag t) { return v4u{(__float_as_uint(a) >> 8) | t.lo, t.hi, (__float_as_uint(b) >> 8) | t.lo, t.hi}; }
__device__ __forceinline__ bool gok2(v4u v, Tag t) { return (v.y == t.hi) & (v.w == t.hi) & (((v.x ^ t.lo) | (v.z ^ t.lo)) < 0x1000000u); }
__device__ __forceinline__ float gval(unsigned lo) { return __uint_as_float(lo << 8); }
#define BLOAD16(off) __builtin_amdgcn_raw_buffer_load_b128(rs, (off), 0, 16)
#define BSTORE16(v, off) __builtin_amdgcn_raw_buffer_store_b128((v), rs, (off), 0, 16)
#define MEMFENCE() asm volatile("" ::: "memory")
#define LGKM0() asm volatile("s_waitcnt lgkmcnt(0)" ::: "memory")
#define VMCNT(n) asm volatile("s_waitcnt vmcnt(" #n ")" ::: "memory")
#define BARRIER() do { LGKM0(); __builtin_amdgcn_s_barrier(); MEMFENCE(); } while (0)
#define LDSP(T, a) ((LAS T*)(uintptr_t)(a))
#define LDS_SPIN(cond) do { for (unsigned spin_ = 0; spin_ < (SPIN_CAP << 7); ++spin_) { if (cond) break; __builtin_amdgcn_s_sleep(1); } MEMFENCE(); } while (0)

__device__ __forceinline__ void dma1(const void* gsrc, unsigned lds_dst) {
  unsigned keep;
  asm volatile("s_mov_b32 %0, m0\n\ts_mov_b32 m0, %2\n\ts_nop 0\n\tglobal_load_lds_dwordx4 %1, off nt\n\ts_mov_b32 m0, %0" : "=&s"(keep) : "v"(gsrc), "s"(lds_dst) : "memory");
}
__device__ __forceinline__ void dma4(unsigned oe, unsigned oo, const char* gb, unsigned lds_dst) {
  unsigned keep;
  asm volatile("s_mov_b32 %0, m0\n\ts_mov_b32 m0, %4\n\ts_nop 0\n\t"
               "global_load_lds_dwordx4 %1, %3 nt\n\tglobal_load_lds_dwordx4 %2, %3 offset:1024 nt\n\t"
               "global_load_lds_dwordx4 %1, %3 offset:2048 nt\n\tglobal_load_lds_dwordx4 %2, %3 offset:3072 nt\n\ts_mov_b32 m0, %0"
               : "=&s"(keep) : "v"(oe), "v"(oo), "s"(gb), "s"(lds_dst) : "memory");
}
template <int R> __device__ __forceinline__ void park4(unsigned oe, unsigned oo, const char* gb) {
  asm volatile("global_load_dwordx4 v[%c3+0:%c3+3], %0, %2 nt\n\tglobal_load_dwordx4 v[%c3+4:%c3+7], %1, %2 offset:1024 nt\n\t"
               "global_load_dwordx4 v[%c3+8:%c3+11], %0, %2 offset:2048 nt\n\tglobal_load_dwordx4 v[%c3+12:%c3+15], %1, %2 offset:3072 nt"
               :: "v"(oe), "v"(oo), "s"(gb), "i"(R) : "memory");
}
template <int R> __device__ __forceinline__ void unpark(unsigned addr) {
  asm volatile("ds_write_b128 %0, v[%c1+0:%c1+3]\n\tds_write_b128 %0, v[%c1+4:%c1+7] offset:1024\n\tds_write_b128 %0, v[%c1+8:%c1+11] offset:2048\n\t"
               "ds_write_b128 %0, v[%c1+12:%c1+15] offset:3072\n\tds_write_b128 %0, v[%c1+16:%c1+19] offset:4096\n\tds_write_b128 %0, v[%c1+20:%c1+23] offset:5120\n\t"
               "ds_write_b128 %0, v[%c1+24:%c1+27] offset:6144\n\tds_write_b128 %0, v[%c1+28:%c1+31] offset:7168\n\ts_waitcnt lgkmcnt(0)"
               :: "v"(addr), "i"(R) : "memory");
}
__device__ __forceinline__ v4f mfma_tr(unsigned a_lo, unsigned a_hi, v8s bb, v4f acc) {
  const v4s a0 = __builtin_amdgcn_ds_read_tr16_b64_v4i16(LDSP(v4s, a_lo));
  const v4s a1 = __builtin_amdgcn_ds_read_tr16_b64_v4i16(LDSP(v4s, a_hi));
  const v8s a = __builtin_shufflevector(a0, a1, 0, 1, 2, 3, 4, 5, 6, 7);
  return __builtin_amdgcn_mfma_f32_16x16x32_bf16(__builtin_bit_cast(v8bf, a), __builtin_bit_cast(v8bf, bb), acc, 0, 0, 0);
}

__global__ void __launch_bounds__(512, 2) k_fused(const u16* __restrict__ x, const u16* __restrict__ Kc, const u16* __restrict__ Vc, const u16* __restrict__ wq,
                                                  const u16* __restrict__ wo, const u16* __restrict__ w1, const u16* __restrict__ w3, const u16* __restrict__ w2,
                                                  u16* __restrict__ out, unsigned char* __restrict__ ws) {
  extern __shared__ __attribute__((aligned(16))) unsigned char lds[];
  const int wave = __builtin_amdgcn_readfirstlane(threadIdx.x >> 6);
  const int wg = blockIdx.x, grp = wg >> 4, sp = wg & 15, b = grp >> 3, kvh = grp & 7;
  const int job = (sp >> 2) ^ (grp & 1), jidx = grp * 4 + (sp & 3);
  const bool pj = job == 0 || (job == 2 && jidx < 56);
  const unsigned ldsbase = (unsigned)(uintptr_t)(LAS unsigned char*)lds;
  const __amdgpu_buffer_rsrc_t rs = __builtin_amdgcn_make_buffer_rsrc(ws, 0, W_END, 0x00020000);

  if (wave < 4) {
    asm volatile("; SPATH_BEGIN" ::: "v247");
    int lane;
    asm volatile("v_mbcnt_lo_u32_b32 %0, -1, 0\n\tv_mbcnt_hi_u32_b32 %0, -1, %0" : "=v"(lane));
    const int n = lane & 15, g = lane >> 4;
    const unsigned ring = ldsbase + L_RING + wave * 32768;
    const unsigned koe = g * 256 + ((n & 8) | ((n & 7) ^ g)) * 16, koo = g * 256 + ((n & 8) | ((n & 7) ^ (4 + g))) * 16;
    const unsigned voe = g * 256 + (n ^ (g << 1)) * 16, voo = g * 256 + (n ^ ((4 + g) << 1)) * 16;
    const int gx = grp & 1, qd = sp >> 2;
    const int c0 = gx ? N1 : N0, c1 = gx ? N0 : N1, c2 = gx ? N3 : N2;
    const int nt = job == 0 ? N0 : job == 1 ? N1 : job == 2 ? N2 : N3;
    const int t0s = 4 * ((qd > 0 ? c0 : 0) + (qd > 1 ? c1 : 0) + (qd > 2 ? c2 : 0)) + (sp & 3) * nt;
    const size_t gbase = (size_t)grp * SK * 256 + (size_t)wave * 4096 + (size_t)t0s * 16384;
    const char* kptr = (const char*)Kc + gbase;
    const char* vptr = (const char*)Vc + gbase;
#define ISSUE_K(t, slot) dma4(koe, koo, kptr + (size_t)(t) * 16384, ring + (slot) * 8192)
#define ISSUE_V(t, slot) dma4(voe, voo, vptr + (size_t)(t) * 16384, ring + (slot) * 8192 + 4096)
#define PARK(r) do { VMCNT(4); park4<RB + 32 * (r)>(koe, koo, kptr + (size_t)(4 + (r)) * 16384); VMCNT(4); park4<RB + 32 * (r) + 16>(voe, voo, vptr + (size_t)(4 + (r)) * 16384); } while (0)
    if (pj) { __builtin_amdgcn_s_barrier(); LDS_SPIN(*LDSP(volatile unsigned, ldsbase + L_MISC + 32 + (wave ? wave : 1) * 4)); }
    ISSUE_K(0, 0); ISSUE_V(0, 0);
    if (!pj) __builtin_amdgcn_s_barrier();
    VMCNT(4); ISSUE_K(1, 1); VMCNT(4); ISSUE_V(1, 1);
    VMCNT(4); ISSUE_K(2, 2); VMCNT(4); ISSUE_V(2, 2);
    VMCNT(4); ISSUE_K(3, 3); VMCNT(4); ISSUE_V(3, 3);
    PARK(0); PARK(1); PARK(2); PARK(3);
    unsigned ka[4], va[8];
#pragma unroll
    for (int t = 0; t < 4; ++t) ka[t] = ring + n * 256 + ((8 * (g & 1)) | ((4 * (g >> 1) + t) ^ (n & 7))) * 16;
    { const unsigned row = lane >> 2;
#pragma unroll
      for (int dt = 0; dt < 8; ++dt) va[dt] = ring + 4096 + row * 256 + ((dt ^ (row & 7)) << 5) + (lane & 3) * 8; }
    const unsigned wa2 = ring + 2 * 8192 + lane * 16, wa3 = wa2 + 8192;
    LDS_SPIN(*LDSP(volatile unsigned, ldsbase + L_MISC + 16));
    v8bf qf[4];
#pragma unroll
    for (int t = 0; t < 4; ++t) qf[t] = __builtin_bit_cast(v8bf, *LDSP(const v4u, ldsbase + L_QL + (n & 3) * 256 + (8 * (g & 1) + 4 * (g >> 1) + t) * 16));
    float m = -INFINITY, l = 0.f;
    v4f O[8];
#pragma unroll
    for (int dt = 0; dt < 8; ++dt) O[dt] = v4f{0, 0, 0, 0};
    for (int j = 0; j < nt; ++j) {
      int slot = j & 3;
      if (j >= 8) {
        if (j + 1 < nt) VMCNT(8); else VMCNT(0);
        if (j + 2 < nt) { ISSUE_K(j + 2, (j + 2) & 3); ISSUE_V(j + 2, (j + 2) & 3); }
      } else if (j >= 2) {
        slot = 2 + (j & 1);
        if (j == 2) { VMCNT(8); ISSUE_K(8, 0); ISSUE_V(8, 0); }
        else if (j == 4) unpark<RB>(wa2);
        else if (j == 5) unpark<RB + 32>(wa3);
        else if (j == 6) { VMCNT(8); ISSUE_K(9, 1); ISSUE_V(9, 1); unpark<RB + 64>(wa2); }
        else if (j == 7) unpark<RB + 96>(wa3);
      }
      if (j == nt - 3 && wave == 0 && lane == 0) *LDSP(volatile unsigned, ldsbase + L_MISC + 28) = 1u;
      const unsigned so = slot * 8192;
      v4u kf[4]; v4s vf[8];
#pragma unroll
      for (int t = 0; t < 4; ++t) kf[t] = *LDSP(const v4u, ka[t] + so);
#pragma unroll
      for (int dt = 0; dt < 8; ++dt) vf[dt] = __builtin_amdgcn_ds_read_tr16_b64_v4i16(LDSP(v4s, va[dt] + so));
      v4f S = {0, 0, 0, 0};
#pragma unroll
      for (int t = 0; t < 4; ++t) S = __builtin_amdgcn_mfma_f32_16x16x32_bf16(__builtin_bit_cast(v8bf, kf[t]), qf[t], S, 0, 0, 0);
      const unsigned a01 = pk2(S[0], S[1]), a23 = pk2(S[2], S[3]);
      const unsigned c01 = pk2(lo16(a01) * SCALE, hi16(a01) * SCALE), c23 = pk2(lo16(a23) * SCALE, hi16(a23) * SCALE);
      const float s0 = lo16(c01), s1 = hi16(c01), s2 = lo16(c23), s3 = hi16(c23);
      const float tm = xrow16_max(fmaxf(fmaxf(s0, s1), fmaxf(s2, s3)));
      if (__any(tm > m)) {
        const float mn = fmaxf(m, tm);
        const float alpha = __builtin_amdgcn_exp2f((m - mn) * LOG2E);
        l *= alpha;
#pragma unroll
        for (int dt = 0; dt < 8; ++dt) O[dt] *= alpha;
        m = mn;
      }
      const float p0 = __builtin_amdgcn_exp2f((s0 - m) * LOG2E), p1 = __builtin_amdgcn_exp2f((s1 - m) * LOG2E);
      const float p2 = __builtin_amdgcn_exp2f((s2 - m) * LOG2E), p3 = __builtin_amdgcn_exp2f((s3 - m) * LOG2E);
      l += (p0 + p1) + (p2 + p3);
      const v2u pp = {pk2(p0, p1), pk2(p2, p3)};
      const v4s pf = __builtin_bit_cast(v4s, pp);
#pragma unroll
      for (int dt = 0; dt < 8; ++dt) O[dt] = __builtin_amdgcn_mfma_f32_16x16x16bf16_1k(vf[dt], pf, O[dt], 0, 0, 0);
      LGKM0();
    }
    l = xrow16_sum(l);
    if (n < 4) {
      const unsigned r = ring + n * 528;
#pragma unroll
      for (int dt = 0; dt < 8; ++dt) *LDSP(v4f, r + 16 + (16 * dt + 4 * g) * 4) = O[dt];
      if (g == 0) *LDSP(v2f, r) = v2f{m, l};
    }
    BARRIER();
    {
      const v2u tg = *LDSP(const v2u, ldsbase + L_MISC);
      const Tag T{tg.x, tg.y};
      v2f ml[4], ov[4];
#pragma unroll
      for (int w = 0; w < 4; ++w) { const unsigned r = ldsbase + L_RING + w * 32768 + wave * 528; ml[w] = *LDSP(const v2f, r); ov[w] = *LDSP(const v2f, r + 16 + lane * 8); }
      const float M = fmaxf(fmaxf(ml[0].x, ml[1].x), fmaxf(ml[2].x, ml[3].x));
      float L = 0, o0 = 0, o1 = 0;
#pragma unroll
      for (int w = 0; w < 4; ++w) { const float wt = __builtin_amdgcn_exp2f((ml[w].x - M) * LOG2E); L = fmaf(wt, ml[w].y, L); o0 = fmaf(wt, ov[w].x, o0); o1 = fmaf(wt, ov[w].y, o1); }
      BSTORE16(gran2(o0, o1, T), W_IN1 + ((grp * 16 + 4 * wave + g) * 16 + sp) * 256 + n * 16);
      if (lane == 0) BSTORE16(gran2(M, L, T), W_ML + (grp * 16 + sp) * 64 + wave * 16);
    }
    asm volatile("; SPATH_END");
    return;
  }

  const int lane = threadIdx.x & 63, n = lane & 15, g = lane >> 4, hw = wave - 4;
  const u64 tk0 = __builtin_amdgcn_s_memrealtime();
#define WAIT_UNTIL(ticks) do { while (__builtin_amdgcn_s_memrealtime() - tk0 < (u64)(ticks)) __builtin_amdgcn_s_sleep(16); } while (0)
  Tag T;
  v4u w[28];
  {
    v2u ep;
    const unsigned char* e = ws + W_EPOCH;
    if (pj) {
      const int mat = jidx >= 28, cbk = jidx - 28 * mat;
      const char* wsrc = job == 0 ? (const char*)wq + jidx * 128 : (const char*)(mat ? w3 : w1) + cbk * 128;
      const unsigned ldw = job == 0 ? 8192u : (unsigned)(FF * 2);
      const unsigned obase = job == 0 ? W_QG + 64 * jidx * 8 : W_HG + (mat * 2 * FF + 64 * cbk) * 8, ostride = job == 0 ? 4096u * 8 : (unsigned)(FF * 8);
      const unsigned stg = hw == 0 ? ldsbase + L_STAGE : ldsbase + L_RING + hw * 32768 + 16384;
      v4u x0, x1;
      { const u16* xp = x + lane * 8;
        asm volatile("global_load_dwordx4 %0, %3, off nt\n\tglobal_load_dwordx4 %1, %3, off offset:1024 nt\n\tglobal_load_dwordx2 %2, %4, off nt" : "=&v"(x0), "=&v"(x1), "=&v"(ep) : "v"(xp), "v"(e) : "memory"); }
      { const char* src = wsrc + (size_t)(128 * hw + (lane >> 3)) * ldw + (lane & 7) * 16;
#pragma unroll
        for (int i = 0; i < 16; ++i) dma1(src + (size_t)i * (8 * ldw), stg + i * 1024); }
      if (hw == 0 && lane < 3) *LDSP(v4u, ldsbase + L_MISC + 16 + lane * 16) = v4u{0, 0, 0, 0};
      BARRIER();
      asm volatile("s_waitcnt vmcnt(16)" : "+v"(x0), "+v"(x1), "+v"(ep) :: "memory");
      {
        float v[2][8];
#pragma unroll
        for (int j = 0; j < 4; ++j) { v[0][2 * j] = lo16(x0[j]); v[0][2 * j + 1] = hi16(x0[j]); v[1][2 * j] = lo16(x1[j]); v[1][2 * j + 1] = hi16(x1[j]); }
#pragma unroll
        for (int bb = 0; bb < 2; ++bb) {
          const float s_ = ((v[bb][0] + v[bb][1]) + (v[bb][2] + v[bb][3])) + ((v[bb][4] + v[bb][5]) + (v[bb][6] + v[bb][7]));
          const float mean = wave_sum(s_) * (1.f / DM);
          float q_ = 0;
#pragma unroll
          for (int j = 0; j < 8; ++j) { const float d = v[bb][j] - mean; q_ = fmaf(d, d, q_); }
          const float var = wave_sum(q_) * (1.f / DM);
          const float mb = rbf(mean);
          const float inv = __builtin_amdgcn_rcpf(rbf(__builtin_amdgcn_sqrtf(rbf(rbf(var) + 1e-5f))));
          v4u o;
#pragma unroll
          for (int j = 0; j < 4; ++j) o[j] = pk2(rbf(v[bb][2 * j] - mb) * inv, rbf(v[bb][2 * j + 1] - mb) * inv);
          const unsigned xa = ldsbase + L_XN + bb * 1024 + (lane >> 2) * 64 + (lane & 1) * 32 + ((lane >> 1) & 1) * 8;
          if (g == hw) { *LDSP(v2u, xa) = v2u{o[0], o[1]}; *LDSP(v2u, xa + 16) = v2u{o[2], o[3]}; }
        }
      }
      asm volatile("s_waitcnt vmcnt(0) lgkmcnt(0)" ::: "memory");
      v4f acc[4];
#pragma unroll
      for (int ct = 0; ct < 4; ++ct) acc[ct] = v4f{0, 0, 0, 0};
      { const unsigned aaddr = stg + (lane >> 2) * 128 + (lane & 3) * 8, baddr = ldsbase + L_XN + (lane & 1) * 1024 + hw * 256 + g * 16;
#pragma unroll
        for (int s_ = 0; s_ < 4; ++s_) { const v8s bb = *LDSP(const v8s, baddr + s_ * 64);
#pragma unroll
          for (int ct = 0; ct < 4; ++ct) acc[ct] = mfma_tr(aaddr + s_ * 4096 + ct * 32, aaddr + s_ * 4096 + 2048 + ct * 32, bb, acc[ct]); } }
      if (n < 2) {
#pragma unroll
        for (int ct = 0; ct < 4; ++ct) *LDSP(v4f, ldsbase + L_QP + hw * 512 + (ct * 8 + g * 2 + n) * 16) = acc[ct];
      }
      LGKM0();
      if (hw != 0) {
        if (lane == 0) *LDSP(volatile unsigned, ldsbase + L_MISC + 32 + hw * 4) = 1u;
      } else {
        T = make_tag(ep);
        for (unsigned spin = 0; spin < (SPIN_CAP << 7); ++spin) { const v4u f = *LDSP(volatile v4u, ldsbase + L_MISC + 32); if (f.y & f.z & f.w) break; }
        MEMFENCE();
        if (n < 8) {
          const unsigned qa = ldsbase + L_QP + ((n >> 1) * 8 + g * 2 + (n & 1)) * 16;
          const v4f q = (*LDSP(const v4f, qa) + *LDSP(const v4f, qa + 512)) + (*LDSP(const v4f, qa + 1024) + *LDSP(const v4f, qa + 1536));
          const unsigned a = obase + (n & 1) * ostride + (16 * (n >> 1) + 4 * g) * 8;
          BSTORE16(gran2(rbf(q[0]), rbf(q[1]), T), a);
          BSTORE16(gran2(rbf(q[2]), rbf(q[3]), T), a + 16);
        }
      }
    } else {
      if (hw == 0) {
        asm volatile("global_load_dwordx2 %0, %1, off nt" : "=&v"(ep) : "v"(e) : "memory");
        if (lane < 3) *LDSP(v4u, ldsbase + L_MISC + 16 + lane * 16) = v4u{0, 0, 0, 0};
      }
      BARRIER();
      if (hw == 2 && job == 1) {
#pragma unroll
        for (int r = 0; r < 28; ++r) w[r] = __builtin_nontemporal_load((const GAS v4u*)(w2 + (size_t)((jidx >> 2) * 112 + g * 28 + r) * DM + (jidx & 3) * 128 + n * 8));
      }
      if (hw == 0) {
        asm volatile("s_waitcnt vmcnt(0)" : "+v"(ep) :: "memory");
        T = make_tag(ep);
        WAIT_UNTIL(T_Q);
      }
    }
    if (hw == 0) {
      if (lane == 0) __builtin_amdgcn_raw_buffer_store_b64(v2u{T.lo, T.hi}, rs, W_ALIVE + wg * 8, 0, 16);
      const unsigned a = W_QG + (b * 4096 + kvh * 512) * 8 + lane * 16;
      v4u v[4];
      bool ok[4] = {false, false, false, false};
      for (unsigned spin = 0; spin < SPIN_CAP; ++spin) {
        MEMFENCE();
#pragma unroll
        for (int i = 0; i < 4; ++i) if (!ok[i]) v[i] = BLOAD16(a + i * 1024);
#pragma unroll
        for (int i = 0; i < 4; ++i) ok[i] = gok2(v[i], T);
        if (__all(ok[0] & ok[1] & ok[2] & ok[3])) break;
        __builtin_amdgcn_s_sleep(4);
      }
#pragma unroll
      for (int i = 0; i < 4; ++i) *LDSP(unsigned, ldsbase + L_QL + i * 256 + lane * 4) = ((v[i].x >> 8) & 0xffffu) | ((v[i].z << 8) & 0xffff0000u);
      if (lane == 0) *LDSP(v2u, ldsbase + L_MISC) = v2u{T.lo, T.hi};
      LGKM0();
      if (lane == 0) *LDSP(volatile unsigned, ldsbase + L_MISC + 16) = 1u;
    } else {
      LDS_SPIN(*LDSP(volatile unsigned, ldsbase + L_MISC + 16));
      const v2u tg = *LDSP(const v2u, ldsbase + L_MISC); T = Tag{tg.x, tg.y};
    }
  }
  if (hw == 2 && job == 1) {
    const int rb = jidx >> 2, cb = jidx & 3;
    const unsigned a = W_HG + (rb * 112 + 2 * (lane < 56 ? lane : 0)) * 8;
    v4u pv[4];
    WAIT_UNTIL(T_DOWN);
    for (unsigned spin = 0; spin < SPIN_CAP; ++spin) {
      if (spin) __builtin_amdgcn_s_sleep(64);
      MEMFENCE();
#pragma unroll
      for (int i = 0; i < 4; ++i) pv[i] = BLOAD16(a + i * FF * 8);
      if (__all(gok2(pv[0], T) & gok2(pv[1], T) & gok2(pv[2], T) & gok2(pv[3], T))) break;
    }
    if (lane < 56) {
#pragma unroll
      for (int bb = 0; bb < 2; ++bb)
        *LDSP(v2f, ldsbase + L_H + bb * 512 + lane * 8) = v2f{rbf(rbf(silu_f(gval(pv[bb].x))) * gval(pv[2 + bb].x)), rbf(rbf(silu_f(gval(pv[bb].z))) * gval(pv[2 + bb].z))};
    }
    LGKM0();
    float z[2][8];
#pragma unroll
    for (int c = 0; c < 8; ++c) z[0][c] = z[1][c] = 0.f;
#pragma unroll
    for (int ch = 0; ch < 7; ++ch) {
      const v4f h0 = *LDSP(const v4f, ldsbase + L_H + (g * 28 + ch * 4) * 4), h1 = *LDSP(const v4f, ldsbase + L_H + 512 + (g * 28 + ch * 4) * 4);
#pragma unroll
      for (int r = 0; r < 4; ++r)
#pragma unroll
        for (int j = 0; j < 4; ++j) { const float wl = lo16(w[ch * 4 + r][j]), wh = hi16(w[ch * 4 + r][j]);
          z[0][2 * j] = fmaf(h0[r], wl, z[0][2 * j]); z[0][2 * j + 1] = fmaf(h0[r], wh, z[0][2 * j + 1]);
          z[1][2 * j] = fmaf(h1[r], wl, z[1][2 * j]); z[1][2 * j + 1] = fmaf(h1[r], wh, z[1][2 * j + 1]); }
    }
#pragma unroll
    for (int bb = 0; bb < 2; ++bb) {
      float t4[4];
#pragma unroll
      for (int j = 0; j < 4; ++j) t4[j] = swap32_add(z[bb][j], z[bb][j + 4]);
      BSTORE16(gran2(swap16_add(t4[0], t4[2]), swap16_add(t4[1], t4[3]), T), W_INZ + ((bb * 32 + 8 * cb + (n >> 1)) * 16 + rb) * 128 + ((n & 1) * 4 + g) * 16);
    }
  }
  if (hw == 3 && wg == 0) {
    WAIT_UNTIL(T_DOWN);
    for (unsigned spin = 0; spin < SPIN_CAP; ++spin) {
      if (spin) __builtin_amdgcn_s_sleep(64);
      MEMFENCE();
      const v4u a0 = BLOAD16(W_ALIVE + lane * 16), a1 = BLOAD16(W_ALIVE + 1024 + lane * 16);
      if (__all(gok2(a0, T) & gok2(a1, T))) break;
    }
    if (lane == 0) {
      const v2u e = __builtin_amdgcn_raw_buffer_load_b64(rs, W_EPOCH, 0, 16);
      const u64 tv = (((u64)e.y << 32) | e.x) + 1;
      __builtin_amdgcn_raw_buffer_store_b64(v2u{(unsigned)tv, (unsigned)(tv >> 32)}, rs, W_EPOCH, 0, 16);
    }
  }
  float xr0 = 0.f, xr1 = 0.f;
  if (job == 3) {
    const unsigned xv = __builtin_nontemporal_load((const GAS unsigned*)(x + jidx * 16 + (lane & 7) * 2));
    xr0 = lo16(xv); xr1 = hi16(xv);
    if (hw == 3) {
      const unsigned a = W_INZ + jidx * 2048 + lane * 16;
      v4u v[2];
      WAIT_UNTIL(T_Z);
      for (unsigned spin = 0; spin < SPIN_CAP; ++spin) {
        if (spin) __builtin_amdgcn_s_sleep(64);
        MEMFENCE();
        v[0] = BLOAD16(a); v[1] = BLOAD16(a + 1024);
        if (__all(gok2(v[0], T) & gok2(v[1], T))) break;
      }
      float z0 = gval(v[0].x) + gval(v[1].x), z1 = gval(v[0].z) + gval(v[1].z);
      z0 += dpp<ROR8>(z0); z1 += dpp<ROR8>(z1);
      z0 = xrow16_sum(z0); z1 = xrow16_sum(z1);
      if (lane < 8) *LDSP(v2f, ldsbase + L_H + lane * 8) = v2f{rbf(silu_f(rbf(z0))), rbf(silu_f(rbf(z1)))};
    }
  }
  unsigned wp[4][8];
  {
    const int row0 = (kvh * 4 + (sp >> 2)) * 128 + (sp & 3) * 32 + 8 * g;
    v4u w_[8];
    for (unsigned spin = 0; spin < (SPIN_CAP << 7); ++spin) { if (*LDSP(volatile unsigned, ldsbase + L_MISC + 28)) break; __builtin_amdgcn_s_sleep(8); }
    MEMFENCE();
#pragma unroll
    for (int i = 0; i < 8; ++i) w_[i] = __builtin_nontemporal_load((const GAS v4u*)(wo + (size_t)(row0 + i) * DM + 128 * hw + 8 * n));
#pragma unroll
    for (int k = 0; k < 4; ++k)
#pragma unroll
      for (int j = 0; j < 4; ++j) { wp[k][2 * j] = __builtin_amdgcn_perm(w_[2 * k + 1][j], w_[2 * k][j], 0x05040100u); wp[k][2 * j + 1] = __builtin_amdgcn_perm(w_[2 * k + 1][j], w_[2 * k][j], 0x07060302u); }
#pragma unroll
    for (int k = 0; k < 4; ++k)
#pragma unroll
      for (int j = 0; j < 8; ++j) asm volatile("" : "+v"(wp[k][j]));
  }
  BARRIER();
  {
    unsigned op[4];
    if (hw == 0) {
      const unsigned a = W_IN1 + (wg * 16 + n) * 256 + g * 64, am = W_ML + (grp * 16 + n) * 64 + (sp >> 2) * 16;
      v4u v[4], ml;
      bool ok = false, mok = false, eager = false;
      for (unsigned spin = 0; spin < SPIN_CAP; ++spin) {
        MEMFENCE();
        const bool ld = !ok & (mok | eager);
        if (!mok) ml = BLOAD16(am);
        if (ld) {
#pragma unroll
          for (int i = 0; i < 4; ++i) v[i] = BLOAD16(a + i * 16);
          ok = gok2(v[0], T) & gok2(v[1], T) & gok2(v[2], T) & gok2(v[3], T);
        }
        mok = gok2(ml, T);
        const u64 miss = __ballot(!mok);
        if (__all(ok & mok)) break;
        eager = __builtin_popcountll(miss) <= 8;
        if (!eager) __builtin_amdgcn_s_sleep(3);
      }
      const float mp = gval(ml.x), lp = gval(ml.z);
      const float M = row_max(mp);
      const float wt = __builtin_amdgcn_exp2f((mp - M) * LOG2E);
      const float invL = __builtin_amdgcn_rcpf(row_sum(wt * lp));
#pragma unroll
      for (int i = 0; i < 4; ++i) op[i] = pk2(row_sum(wt * gval(v[i].x)) * invL, row_sum(wt * gval(v[i].z)) * invL);
      if (n == 0) *LDSP(v4u, ldsbase + L_OP + g * 16) = v4u{op[0], op[1], op[2], op[3]};
      LGKM0();
      if (lane == 0) *LDSP(volatile unsigned, ldsbase + L_MISC + 20) = 1u;
    } else {
      LDS_SPIN(*LDSP(volatile unsigned, ldsbase + L_MISC + 20));
      const v4u o4 = *LDSP(const v4u, ldsbase + L_OP + g * 16);
      op[0] = o4.x; op[1] = o4.y; op[2] = o4.z; op[3] = o4.w;
    }
    float y[8];
#pragma unroll
    for (int c = 0; c < 8; ++c) { float acc = 0.f;
#pragma unroll
      for (int k = 0; k < 4; ++k) acc = dot2(wp[k][c], op[k], acc);
      y[c] = acc; }
    float t4[4];
#pragma unroll
    for (int j = 0; j < 4; ++j) t4[j] = swap32_add(y[j], y[j + 4]);
    BSTORE16(gran2(swap16_add(t4[0], t4[2]), swap16_add(t4[1], t4[3]), T), W_INY + ((b * 32 + 8 * hw + (n >> 1)) * 128 + (wg & 127)) * 128 + ((n & 1) * 4 + g) * 16);
  }
  if (job == 3) {
    const unsigned a = W_INY + jidx * 16384 + hw * 4096 + lane * 16;
    v4u v[4];
    bool ok[4] = {false, false, false, false};
    for (unsigned spin = 0; spin < SPIN_CAP; ++spin) {
      MEMFENCE();
#pragma unroll
      for (int i = 0; i < 4; ++i) if (!ok[i]) v[i] = BLOAD16(a + i * 1024);
#pragma unroll
      for (int i = 0; i < 4; ++i) ok[i] = gok2(v[i], T);
      if (__all(ok[0] & ok[1] & ok[2] & ok[3])) break;
    }
    float s0 = (gval(v[0].x) + gval(v[1].x)) + (gval(v[2].x) + gval(v[3].x)), s1 = (gval(v[0].z) + gval(v[1].z)) + (gval(v[2].z) + gval(v[3].z));
    s0 += dpp<ROR8>(s0); s1 += dpp<ROR8>(s1);
    s0 = xrow16_sum(s0); s1 = xrow16_sum(s1);
    if (lane < 8) *LDSP(v2f, ldsbase + L_QP + hw * 64 + lane * 8) = v2f{s0, s1};
    LGKM0();
    unsigned old = 0;
    if (lane == 0) old = __hip_atomic_fetch_add(LDSP(unsigned, ldsbase + L_MISC + 24), 1u, __ATOMIC_RELAXED, __HIP_MEMORY_SCOPE_WORKGROUP);
    old = __builtin_amdgcn_readfirstlane(old);
    MEMFENCE();
    if (old == 3 && lane < 8) {
      const unsigned pa = ldsbase + L_QP + lane * 8;
      const v2f t = (*LDSP(const v2f, pa) + *LDSP(const v2f, pa + 64)) + (*LDSP(const v2f, pa + 128) + *LDSP(const v2f, pa + 192));
      const v2f gt = *LDSP(const v2f, ldsbase + L_H + lane * 8);
      *(GAS unsigned*)(out + jidx * 16 + lane * 2) = pk2(xr0 + rbf(rbf(t.x) * gt.x), xr1 + rbf(rbf(t.y) * gt.y));
    }
  }
}

extern "C" void kernel_launch(void* const* d_in, const int* in_sizes, int n_in, void* d_out, int out_size, void* d_ws, size_t ws_size, hipStream_t stream) {
  (void)hipFuncSetAttribute((const void*)k_fused, hipFuncAttributeMaxDynamicSharedMemorySize, LDS_BYTES);
  k_fused<<<NWG, 512, LDS_BYTES, stream>>>((const u16*)d_in[0], (const u16*)d_in[1], (const u16*)d_in[2], (const u16*)d_in[3], (const u16*)d_in[4],
                                           (const u16*)d_in[6], (const u16*)d_in[7], (const u16*)d_in[8], (u16*)d_out, (unsigned char*)d_ws);
}
```
